```python
import torch

def _softmax(x, axis=-1):
    return torch.softmax(x, dim=axis)

def _gelu(x):
    return torch.nn.functional.gelu(x, approximate='tanh')

def _silu(x):
    return torch.nn.functional.silu(x)

def _sigmoid(x):
    return torch.sigmoid(x)

def _layernorm(x, axis=-1, eps=1e-05):
    m = x.mean(dim=axis, keepdim=True)
    v = x.var(dim=axis, keepdim=True, unbiased=False)
    return (x - m) / torch.sqrt(v + eps)

def _scatter_add(tgt, idx, src, axis=0):
    return tgt.clone().index_add_(axis, idx, src)

def _segment_sum(src, seg, n):
    out = torch.zeros((n,) + src.shape[1:], dtype=src.dtype)
    return out.index_add_(0, seg, src)

def _pad(x, pw):
    flat = tuple((v for pair in reversed(pw) for v in pair))
    return torch.nn.functional.pad(x, flat)
kernel_name = 'adaLN_cond__sliding_block_cached__swiglu_ffn__decode'

def setup_inputs():

    _w = lambda a: (lambda t: t.to(torch.bfloat16) if t.is_floating_point() else t)(torch.as_tensor(a))

    def _A_setup():
        B, Sq, D = (8, 1, 512)
        Hq, Hkv, Sk, Dh = (32, 8, 8192, 128)
        assert Hq % Hkv == 0, (Hq, Hkv)
        return {'x': _w((lambda *s: torch.randn(*s, dtype=torch.bfloat16) * 0.05)(B, Sq, D)), 'k_cache': _w((lambda *s: torch.randn(*s, dtype=torch.bfloat16) * 3.2)(B, Hkv, Sk, Dh)), 'v_cache': _w((lambda *s: torch.randn(*s, dtype=torch.bfloat16) * 0.4)(B, Hkv, Sk, Dh)), 'wq': _w((lambda *s: torch.randn(*s, dtype=torch.bfloat16) * 3.2)(D, Hq * Dh) * D ** (-0.5)), 'wo': _w((lambda *s: torch.randn(*s, dtype=torch.bfloat16) * 0.4)(Hq * Dh, D) * (Hq * Dh) ** (-0.5))}
    _A_in = _A_setup()

    def _B_setup():
        B, S, D, F = (8, 1, 512, 1792)
        return {'x': _w((lambda *s: torch.randn(*s, dtype=torch.bfloat16) * 0.05)(B, S, D)), 'w_gate': _w((lambda *s: torch.randn(*s, dtype=torch.bfloat16) * 0.05)(D, F)), 'w_up': _w((lambda *s: torch.randn(*s, dtype=torch.bfloat16) * 0.05)(D, F)), 'w_down': _w((lambda *s: torch.randn(*s, dtype=torch.bfloat16) * 0.05)(F, D))}
    _B_in = _B_setup()
    return {'A_x': _A_in['x'], 'A_k_cache': _A_in['k_cache'], 'A_v_cache': _A_in['v_cache'], 'A_wq': _A_in['wq'], 'A_wo': _A_in['wo'], 'B_x': _B_in['x'], 'B_w_gate': _B_in['w_gate'], 'B_w_up': _B_in['w_up'], 'B_w_down': _B_in['w_down'], 'c': _w((lambda *s: torch.randn(*s, dtype=torch.bfloat16) * 0.1)(8, 512) * 8.0), 'w_mod': _w((lambda *s: torch.randn(*s, dtype=torch.bfloat16) * 0.1)(512, 6 * 512) * 512 ** (-0.5) * 8.0)}

def reference(A_x, A_k_cache, A_v_cache, A_wq, A_wo, B_x, B_w_gate, B_w_up, B_w_down, c, w_mod):

    def _A_ref(x, k_cache, v_cache, wq, wo):
        B, Sq, D = x.shape
        Hq, Dh = (32, 128)
        Hkv, Sk = (k_cache.shape[1], k_cache.shape[2])
        W = min(4096, max(256, Sk // 4))
        rep = Hq // Hkv
        q = (lambda x, *d: x.permute(*d))((x @ wq).reshape(B, Sq, Hq, Dh), 0, 2, 1, 3)
        k = (lambda x, n, axis: torch.repeat_interleave(x, n, dim=axis))(k_cache[:, :, Sk - W:, :], rep, axis=1)
        v = (lambda x, n, axis: torch.repeat_interleave(x, n, dim=axis))(v_cache[:, :, Sk - W:, :], rep, axis=1)
        scores = torch.einsum('bhqd,bhkd->bhqk', q, k) * Dh ** (-0.5)
        p = _softmax(scores, axis=-1)
        o = torch.einsum('bhqk,bhkd->bhqd', p, v)
        return (lambda x, *d: x.permute(*d))(o, 0, 2, 1, 3).reshape(B, Sq, Hq * Dh) @ wo

    def _B_ref(x, w_gate, w_up, w_down):
        g = _silu(x @ w_gate)
        u = x @ w_up
        return g * u @ w_down
    x = A_x
    mod = (_silu(c) @ w_mod).reshape(c.shape[0], 6, 1, -1)
    sa, ca, ga, sb, cb, gb = (mod[:, 0].clone(), mod[:, 1].clone(), mod[:, 2].clone(), mod[:, 3].clone(), mod[:, 4].clone(), mod[:, 5].clone())
    h = _layernorm(x, axis=-1) * (1 + ca) + sa
    x = x + ga * _A_ref(h, A_k_cache, A_v_cache, A_wq, A_wo)
    h = _layernorm(x, axis=-1) * (1 + cb) + sb
    x = x + gb * _B_ref(h, B_w_gate, B_w_up, B_w_down)
    return x
```

```cpp
#include <hip/hip_runtime.h>
#include <hip/hip_bf16.h>
#define MIMIC 0
typedef unsigned short u16;
__device__ __forceinline__ float bf2f(u16 b) { return __uint_as_float(((unsigned)b) << 16); }
__device__ __forceinline__ u16 f2bf(float f) { unsigned u = __float_as_uint(f); u += 0x7fffu + ((u >> 16) & 1u); return (u16)(u >> 16); }
__device__ __forceinline__ float rb(float f) {
  return f;
}
constexpr int B = 8, D = 512, HQ = 32, HKV = 8, SK = 8192, DH = 128, W = 2048, F = 1792;
constexpr int NSPLIT = 8;

__global__ void k_mod(const u16* c, const u16* wmod, float* mod) {
  __shared__ float sc[B][D];
  __shared__ float red[8][B][64];
  int tid = threadIdx.y * 64 + threadIdx.x;
  for (int i = tid; i < B * D; i += 512) { float v = bf2f(c[i]); sc[i / D][i % D] = rb(v / (1.f + __expf(-v))); }
  __syncthreads();
  int n = blockIdx.x * 64 + threadIdx.x;
  float acc[B] = {0};
  for (int k = threadIdx.y * 64; k < threadIdx.y * 64 + 64; ++k) {
    float w = bf2f(wmod[(size_t)k * 3072 + n]);
#pragma unroll
    for (int b = 0; b < B; ++b) acc[b] += sc[b][k] * w;
  }
#pragma unroll
  for (int b = 0; b < B; ++b) red[threadIdx.y][b][threadIdx.x] = acc[b];
  __syncthreads();
  { int b = threadIdx.y; float s = 0; for (int y = 0; y < 8; ++y) s += red[y][b][threadIdx.x]; mod[b * 3072 + n] = rb(s); }
}

__device__ void ln_mod(const float* xr  , const float* mod, int sidx, int cidx, float* hout  , int tid) {
  int w = tid >> 6, l = tid & 63;
  float v[8]; float s = 0;
  for (int j = 0; j < 8; ++j) { v[j] = xr[w * D + l + 64 * j]; s += v[j]; }
  for (int o = 32; o; o >>= 1) s += __shfl_xor(s, o);
  float mean = s / D; float q = 0;
  for (int j = 0; j < 8; ++j) { float d = v[j] - mean; q += d * d; }
  for (int o = 32; o; o >>= 1) q += __shfl_xor(q, o);
  float var = rb(q / D); float m = rb(mean);
  float den = rb(sqrtf(rb(var + 1e-5f)));
  for (int j = 0; j < 8; ++j) {
    int d = l + 64 * j;
    float n = rb(rb(v[j] - m) / den);
    float cc = rb(1.f + mod[w * 3072 + cidx * D + d]);
    hout[w * D + d] = rb(rb(n * cc) + mod[w * 3072 + sidx * D + d]);
  }
}

__global__ void k_q(const u16* x, const float* mod, const u16* wq, float* q) {
  __shared__ float xs[B * D];
  __shared__ float h[B * D];
  __shared__ float red[8][B][64];
  int tid = threadIdx.y * 64 + threadIdx.x;
  for (int i = tid; i < B * D; i += 512) xs[i] = bf2f(x[i]);
  __syncthreads();
  ln_mod(xs, mod, 0, 1, h, tid);
  __syncthreads();
  int n = blockIdx.x * 64 + threadIdx.x;
  float acc[B] = {0};
  for (int k = threadIdx.y * 64; k < threadIdx.y * 64 + 64; ++k) {
    float w = bf2f(wq[(size_t)k * 4096 + n]);
#pragma unroll
    for (int b = 0; b < B; ++b) acc[b] += h[b * D + k] * w;
  }
#pragma unroll
  for (int b = 0; b < B; ++b) red[threadIdx.y][b][threadIdx.x] = acc[b];
  __syncthreads();
  { int b = threadIdx.y; float s = 0; for (int y = 0; y < 8; ++y) s += red[y][b][threadIdx.x]; q[b * 4096 + n] = rb(s); }
}

__global__ void k_scores(const float* q, const u16* kc, float* sc) {
  __shared__ float qs[4][DH];
  int b = blockIdx.z, kvh = blockIdx.y;
  for (int i = threadIdx.x; i < 512; i += 256) qs[i / DH][i % DH] = q[b * 4096 + kvh * 512 + i];
  __syncthreads();
  int key = blockIdx.x * 256 + threadIdx.x;
  const uint4* kr = (const uint4*)(kc + ((size_t)(b * HKV + kvh) * SK + (SK - W) + key) * DH);
  float acc[4] = {0};
  for (int j = 0; j < 16; ++j) {
    uint4 v = kr[j];
    unsigned u[4] = {v.x, v.y, v.z, v.w};
    for (int t = 0; t < 4; ++t) {
      float lo = __uint_as_float(u[t] << 16), hi = __uint_as_float(u[t] & 0xffff0000u);
      for (int g = 0; g < 4; ++g) acc[g] += lo * qs[g][j * 8 + 2 * t] + hi * qs[g][j * 8 + 2 * t + 1];
    }
  }
  for (int g = 0; g < 4; ++g) sc[((size_t)(b * HQ + kvh * 4 + g)) * W + key] = rb(rb(acc[g]) * 0.08838834764831845f);
}
__global__ void k_pv(const float* sc, const u16* vc, float* o) {
  __shared__ float p[W];
  __shared__ float red[256];
  __shared__ float acc2[2][DH];
  int b = blockIdx.y, h = blockIdx.x, kvh = h / 4, tid = threadIdx.x;
  const float* s = sc + (size_t)(b * HQ + h) * W;
  float m = -INFINITY;
  for (int i = tid; i < W; i += 256) m = fmaxf(m, s[i]);
  red[tid] = m; __syncthreads();
  for (int o2 = 128; o2; o2 >>= 1) { if (tid < o2) red[tid] = fmaxf(red[tid], red[tid + o2]); __syncthreads(); }
  m = red[0]; __syncthreads();
  float l = 0;
  for (int i = tid; i < W; i += 256) { float e = __expf(s[i] - m); p[i] = e; l += e; }
  red[tid] = l; __syncthreads();
  for (int o2 = 128; o2; o2 >>= 1) { if (tid < o2) red[tid] += red[tid + o2]; __syncthreads(); }
  l = red[0]; __syncthreads();
  for (int i = tid; i < W; i += 256) p[i] = rb(p[i] / l);
  __syncthreads();
  int d = tid & 127, half = tid >> 7;
  const u16* vr = vc + ((size_t)(b * HKV + kvh) * SK + (SK - W)) * DH;
  float a = 0;
  for (int k = half * 1024; k < half * 1024 + 1024; ++k) a += p[k] * bf2f(vr[(size_t)k * DH + d]);
  acc2[half][d] = a; __syncthreads();
  if (tid < 128) o[b * 4096 + h * DH + tid] = rb(acc2[0][tid] + acc2[1][tid]);
}
__global__ void k_wo(const float* o, const u16* wo, const u16* x, const float* mod, float* x1) {
  __shared__ float red[8][B][64];
  int n = blockIdx.x * 64 + threadIdx.x;
  float acc[B] = {0};
  for (int k = threadIdx.y * 512; k < threadIdx.y * 512 + 512; ++k) {
    float w = bf2f(wo[(size_t)k * D + n]);
#pragma unroll
    for (int b = 0; b < B; ++b) acc[b] += o[b * 4096 + k] * w;
  }
#pragma unroll
  for (int b = 0; b < B; ++b) red[threadIdx.y][b][threadIdx.x] = acc[b];
  __syncthreads();
  { int b = threadIdx.y; float s = 0; for (int y = 0; y < 8; ++y) s += red[y][b][threadIdx.x];
    float a = rb(s); x1[b * D + n] = rb(bf2f(x[b * D + n]) + rb(mod[b * 3072 + 2 * D + n] * a)); }
}
__global__ void k_gu(const float* x1, const float* mod, const u16* wg, const u16* wu, float* act) {
  __shared__ float xs[B * D];
  __shared__ float h[B * D];
  __shared__ float red[2][8][B][64];
  int tid = threadIdx.y * 64 + threadIdx.x;
  for (int i = tid; i < B * D; i += 512) xs[i] = x1[i];
  __syncthreads();
  ln_mod(xs, mod, 3, 4, h, tid);
  __syncthreads();
  int n = blockIdx.x * 64 + threadIdx.x;
  float ag[B] = {0}, au[B] = {0};
  for (int k = threadIdx.y * 64; k < threadIdx.y * 64 + 64; ++k) {
    float g = bf2f(wg[(size_t)k * F + n]), u = bf2f(wu[(size_t)k * F + n]);
#pragma unroll
    for (int b = 0; b < B; ++b) { ag[b] += h[b * D + k] * g; au[b] += h[b * D + k] * u; }
  }
#pragma unroll
  for (int b = 0; b < B; ++b) { red[0][threadIdx.y][b][threadIdx.x] = ag[b]; red[1][threadIdx.y][b][threadIdx.x] = au[b]; }
  __syncthreads();
  { int b = threadIdx.y; float g = 0, u = 0; for (int y = 0; y < 8; ++y) { g += red[0][y][b][threadIdx.x]; u += red[1][y][b][threadIdx.x]; }
    g = rb(g); u = rb(u); float sg = rb(g / (1.f + __expf(-g))); act[b * F + n] = rb(sg * u); }
}
__global__ void k_down(const float* act, const u16* wd, const float* x1, const float* mod, u16* out) {
  __shared__ float red[8][B][64];
  int n = blockIdx.x * 64 + threadIdx.x;
  float acc[B] = {0};
  for (int k = threadIdx.y * 224; k < threadIdx.y * 224 + 224; ++k) {
    float w = bf2f(wd[(size_t)k * D + n]);
#pragma unroll
    for (int b = 0; b < B; ++b) acc[b] += act[b * F + k] * w;
  }
#pragma unroll
  for (int b = 0; b < B; ++b) red[threadIdx.y][b][threadIdx.x] = acc[b];
  __syncthreads();
  { int b = threadIdx.y; float s = 0; for (int y = 0; y < 8; ++y) s += red[y][b][threadIdx.x];
    float a = rb(s); out[b * D + n] = f2bf(x1[b * D + n] + rb(mod[b * 3072 + 5 * D + n] * a)); }
}

extern "C" void kernel_launch(void* const* d_in, const int* in_sizes, int n_in, void* d_out, int out_size, void* d_ws, size_t ws_size, hipStream_t stream) {
  const u16* x = (const u16*)d_in[0]; const u16* kc = (const u16*)d_in[1]; const u16* vc = (const u16*)d_in[2];
  const u16* wq = (const u16*)d_in[3]; const u16* wo = (const u16*)d_in[4];
  const u16* wg = (const u16*)d_in[6]; const u16* wu = (const u16*)d_in[7]; const u16* wd = (const u16*)d_in[8];
  const u16* c = (const u16*)d_in[9]; const u16* wmod = (const u16*)d_in[10];
  float* ws = (float*)d_ws;
  float* mod = ws; float* q = mod + B * 3072; float* sc = q + B * 4096; float* o = sc + B * HQ * W; float* x1 = o + B * 4096; float* act = x1 + B * D;
  k_mod<<<48, dim3(64, 8), 0, stream>>>(c, wmod, mod);
  k_q<<<64, dim3(64, 8), 0, stream>>>(x, mod, wq, q);
  k_scores<<<dim3(W / 256, HKV, B), 256, 0, stream>>>(q, kc, sc);
  k_pv<<<dim3(HQ, B), 256, 0, stream>>>(sc, vc, o);
  k_wo<<<8, dim3(64, 8), 0, stream>>>(o, wo, x, mod, x1);
  k_gu<<<28, dim3(64, 8), 0, stream>>>(x1, mod, wg, wu, act);
  k_down<<<8, dim3(64, 8), 0, stream>>>(act, wd, x1, mod, (u16*)d_out);
}
```

```cpp
#define DIAG 0
#include <hip/hip_runtime.h>
#pragma clang fp contract(off)
#define QDELAY 8
#define HDELAY 4
#define FGATE 3
#define WGATE 1
#define PSLEEP 0
#define TAILGATE 0

typedef unsigned short u16;
typedef unsigned v4u __attribute__((ext_vector_type(4)));
typedef unsigned v2u __attribute__((ext_vector_type(2)));
typedef float f32x4 __attribute__((ext_vector_type(4)));
typedef float f32x2 __attribute__((ext_vector_type(2)));
typedef short s16x4 __attribute__((ext_vector_type(4)));
typedef __bf16 bf16x8 __attribute__((ext_vector_type(8)));
typedef __bf16 bf16x2 __attribute__((ext_vector_type(2)));
#define GAS __attribute__((address_space(1)))
#define LAS __attribute__((address_space(3)))
#define DI __device__ __forceinline__

constexpr int SK = 8192, WIN = 2048, FF = 1792;
constexpr int NTHREADS = 768;

constexpr int WS_EPOCH = 0;
constexpr int WS_MP = 4096;
constexpr int WS_QP = WS_MP + 4 * 8 * 3072 * 4;
constexpr int WS_SC = WS_QP + 4 * 8 * 4096 * 4;
constexpr int WS_P1S = WS_SC + 8 * 512 * 8;
constexpr int WS_P1O = WS_P1S + 8 * 32 * 4 * 16;
constexpr int WS_P2 = WS_P1O + 8 * 32 * 4 * 512;
constexpr int WS_X1 = WS_P2 + 8 * 16 * 32 * 128;
constexpr int WS_P3 = WS_X1 + 8 * 512 * 4;
constexpr int WS_END = WS_P3 + 8 * 16 * 56 * 128;
constexpr int WS_STAMP = 8 * 1024 * 1024;

constexpr int L_CTRL = 0;
constexpr int L_Q = 64;
constexpr int L_ST = L_Q + 4 * 272;
constexpr int L_PART = L_ST + 4 * 2 * 12 * 4;
constexpr int L_A0 = L_PART + 9 * 4 * 528;
constexpr int L_A1 = L_A0 + 8 * 272;
constexpr int L_RED = L_A1 + 4 * 1040;
constexpr int L_WM = L_RED + 8 * 1024;
constexpr int L_WW = L_WM + 16 * 1088;
constexpr int WQ_BYTES = 64 * 1088 + 16 * 128;
constexpr int L_WR = L_WW + WQ_BYTES;
constexpr int LDS_BYTES = L_WR + 2 * 16 * 1088;
static_assert(L_WW + 8 * 12544 <= LDS_BYTES, "tail weights");
static_assert(LDS_BYTES <= 160 * 1024 && (L_PART % 16) == 0 && (L_A0 % 16) == 0 && (L_WM % 16) == 0 && (L_WW % 16) == 0 && (L_RED % 16) == 0, "lds");

constexpr unsigned SPIN_CAP = 400000u;
constexpr float LOG2E = 1.4426950408889634f;

DI float bflo(unsigned u) { return __uint_as_float(u << 16); }
DI float bfhi(unsigned u) { return __uint_as_float(u & 0xffff0000u); }
DI unsigned pk(float lo, float hi) { bf16x2 v = {(__bf16)lo, (__bf16)hi}; return __builtin_bit_cast(unsigned, v); }
DI float rb(float x) { return (float)(__bf16)x; }
DI float silu(float v) { return v * __builtin_amdgcn_rcpf(1.f + __builtin_amdgcn_exp2f(-LOG2E * v)); }
template <int CTRL> DI float dpp(float x) { return __builtin_bit_cast(float, __builtin_amdgcn_mov_dpp(__builtin_bit_cast(int, x), CTRL, 0xf, 0xf, true)); }
template <int CTRL> DI unsigned dppu(unsigned x) { return (unsigned)__builtin_amdgcn_mov_dpp((int)x, CTRL, 0xf, 0xf, true); }
DI float x16sum(float x) { auto s = __builtin_amdgcn_permlane16_swap(__float_as_uint(x), __float_as_uint(x), false, false); return __uint_as_float(s[0]) + __uint_as_float(s[1]); }
DI float x32sum(float x) { auto s = __builtin_amdgcn_permlane32_swap(__float_as_uint(x), __float_as_uint(x), false, false); return __uint_as_float(s[0]) + __uint_as_float(s[1]); }
DI float x16max(float x) { auto s = __builtin_amdgcn_permlane16_swap(__float_as_uint(x), __float_as_uint(x), false, false); return fmaxf(__uint_as_float(s[0]), __uint_as_float(s[1])); }
DI float x32max(float x) { auto s = __builtin_amdgcn_permlane32_swap(__float_as_uint(x), __float_as_uint(x), false, false); return fmaxf(__uint_as_float(s[0]), __uint_as_float(s[1])); }
DI float wavesum(float x) { x += dpp<0xB1>(x); x += dpp<0x4E>(x); x += dpp<0x141>(x); x += dpp<0x128>(x); return x32sum(x16sum(x)); }
DI f32x4 mfma32(v4u a, v4u b, f32x4 c) { return __builtin_amdgcn_mfma_f32_16x16x32_bf16(__builtin_bit_cast(bf16x8, a), __builtin_bit_cast(bf16x8, b), c, 0, 0, 0); }
DI f32x4 mfma16(v2u a, v2u b, f32x4 c) { return __builtin_amdgcn_mfma_f32_16x16x16bf16_1k(__builtin_bit_cast(s16x4, a), __builtin_bit_cast(s16x4, b), c, 0, 0, 0); }
DI v2u trread(LAS unsigned char* p) { return __builtin_bit_cast(v2u, __builtin_amdgcn_ds_read_tr16_b64_v4i16((LAS s16x4*)p)); }
DI v4u trread2(LAS unsigned char* p0, LAS unsigned char* p1) { const v2u lo = trread(p0), hi = trread(p1); return v4u{lo.x, lo.y, hi.x, hi.y}; }
DI void glds(const void* gsrc, unsigned lds_dst) {
  unsigned keep;
  asm volatile("s_mov_b32 %0, m0\n\ts_mov_b32 m0, %2\n\ts_nop 0\n\tglobal_load_lds_dwordx4 %1, off nt\n\ts_mov_b32 m0, %0" : "=&s"(keep) : "v"(gsrc), "s"(lds_dst) : "memory");
}
DI void glds_run(const u16* src, int stride, unsigned dst, int dstride, int n) {
#pragma unroll 1
  for (int i = 0; i < n; ++i) { glds(src, dst); src += stride; dst += dstride; }
}
DI void glds_run_thin(const u16* src, int stride, unsigned dst, int dstride, int n) {
#pragma unroll 1
  for (int i = 0; i < n; ++i) { glds(src, dst); src += stride; dst += dstride; asm volatile("s_waitcnt vmcnt(1)" ::: "memory"); }
}
#define VM_WAIT(n) asm volatile("s_waitcnt vmcnt(" #n ")" ::: "memory")
#define BAR() do { asm volatile("s_waitcnt lgkmcnt(0)" ::: "memory"); __builtin_amdgcn_s_barrier(); asm volatile("" ::: "memory"); } while (0)
#define MEMBAR() asm volatile("" ::: "memory")
#define SB() __builtin_amdgcn_sched_barrier(0)
typedef __amdgpu_buffer_rsrc_t rsrc_t;
DI v4u ldx(rsrc_t r, int off) { return __builtin_amdgcn_raw_buffer_load_b128(r, off, 0, 16); }
DI void stx(rsrc_t r, int off, v4u v) { __builtin_amdgcn_raw_buffer_store_b128(v, r, off, 0, 16); }
constexpr unsigned TM = 0xfu;
DI v4u tag8(f32x4 v, unsigned t8) {
  v4u u = __builtin_bit_cast(v4u, v);
  return v4u{(u.x & ~TM) | t8, (u.y & ~TM) | t8, (u.z & ~TM) | t8, (u.w & ~TM) | t8};
}
DI v4u tag8r(f32x4 v, unsigned t8) { return tag8(__builtin_bit_cast(f32x4, __builtin_bit_cast(v4u, v) + 8u), t8); }
DI f32x4 asf(v4u u) { return __builtin_bit_cast(f32x4, u); }
DI f32x4 sel(bool c, f32x4 a, f32x4 b) { return f32x4{c ? a[0] : b[0], c ? a[1] : b[1], c ? a[2] : b[2], c ? a[3] : b[3]}; }
DI void lds_spin(volatile LAS unsigned* w, unsigned want) {
#pragma unroll 1
  for (unsigned s = 0; s < 16u * SPIN_CAP; ++s) { if (*w >= want) break; __builtin_amdgcn_s_sleep(1); }
  MEMBAR();
}
template <int N, int STR> DI void poll_masked(rsrc_t rs, int off, unsigned tg, unsigned mask, v4u (&a)[N]) {
#pragma unroll 1
  for (unsigned s = 0; s < SPIN_CAP; ++s) {
    MEMBAR();
    unsigned bad = 0;
#pragma unroll
    for (int i = 0; i < N; ++i) a[i] = ldx(rs, off + i * STR);
#pragma unroll
    for (int i = 0; i < N; ++i) { a[i] ^= tg; bad |= a[i].x | a[i].y | a[i].z | a[i].w; }
    if (__all((bad & mask) == 0)) break;
    if (PSLEEP) __builtin_amdgcn_s_sleep(PSLEEP);
  }
}
#define STAMP(i) do {} while (0)
#define STAMP_INIT() do {} while (0)
#define EXIT() return
constexpr int LDS_ALLOC = LDS_BYTES;

DI void ln_stats(const float (&v)[8], float& m, float& inv) {
  const float mean = wavesum(((v[0] + v[1]) + (v[2] + v[3])) + ((v[4] + v[5]) + (v[6] + v[7]))) * (1.f / 512.f);
  float q0 = 0.f, q1 = 0.f;
#pragma unroll
  for (int j = 0; j < 4; ++j) { const float d0 = v[j] - mean, d1 = v[4 + j] - mean; q0 = fmaf(d0, d0, q0); q1 = fmaf(d1, d1, q1); }
  const float var = rb(wavesum(q0 + q1) * (1.f / 512.f));
  m = rb(mean);
  inv = __builtin_amdgcn_rcpf(rb(__builtin_amdgcn_sqrtf(rb(var + 1e-5f))));
}
DI void mod_half(LAS unsigned char* img, LAS unsigned char* act, rsrc_t rs, int off, int hf, unsigned t8, int lane) {
  const int q = (lane & 15) >> 2;
  v4u wf[2][4], bf[4];
#pragma unroll
  for (int st = 0; st < 4; ++st) bf[st] = *(LAS v4u*)(act + (lane & 7) * 272 + st * 64 + (lane >> 4) * 16);
#pragma unroll
  for (int ct = 0; ct < 2; ++ct) {
    LAS unsigned char* p = img + (lane >> 4) * 1088 + q * 128 + hf * 64 + ((ct ^ (q >> 1)) * 32) + 8 * (lane & 3);
#pragma unroll
    for (int st = 0; st < 4; ++st) wf[ct][st] = trread2(p + st * 4352, p + st * 4352 + 512);
  }
  SB();
  f32x4 a0 = {0.f, 0.f, 0.f, 0.f}, a1 = {0.f, 0.f, 0.f, 0.f};
#pragma unroll
  for (int st = 0; st < 4; ++st) { a0 = mfma32(wf[0][st], bf[st], a0); a1 = mfma32(wf[1][st], bf[st], a1); }
  stx(rs, off + hf * 128 + (lane & 7) * 12288 + (lane & 8) * 8 + (lane >> 4) * 16, tag8r(sel(lane & 8, a1, a0), t8));
}

__global__ void __launch_bounds__(NTHREADS, 3)
fused_block(const u16* __restrict__ x, const u16* __restrict__ kc, const u16* __restrict__ vc, const u16* __restrict__ wq, const u16* __restrict__ wo,
            const u16* __restrict__ wgate, const u16* __restrict__ wup, const u16* __restrict__ wdown, const u16* __restrict__ cvec, const u16* __restrict__ wmod,
            u16* __restrict__ out, unsigned char* ws) {
  extern __shared__ __attribute__((aligned(16))) unsigned char smem[];
  LAS unsigned char* L = (LAS unsigned char*)smem;
  volatile LAS unsigned* ctrl = (volatile LAS unsigned*)(L + L_CTRL);
  const int wg = blockIdx.x, lane = threadIdx.x & 63, wave = __builtin_amdgcn_readfirstlane(threadIdx.x >> 6);
  const int kvh = wg & 7, b = (wg >> 3) & 7, sp = wg >> 6;
  const bool service = sp == 0;
  const rsrc_t rs = __builtin_amdgcn_make_buffer_rsrc(ws, 0, WS_END, 0x00020000);
  const unsigned lbase = (unsigned)(size_t)L;
  STAMP_INIT();
  STAMP(0);
  if (threadIdx.x < 8 && threadIdx.x != 4) ctrl[threadIdx.x] = 0u;
  unsigned tag = 0;
  const unsigned long long dg0 = __builtin_amdgcn_s_memrealtime(); unsigned long long dgx = dg0;
#define DG(n) do { if (DIAG == (n)) dgx = __builtin_amdgcn_s_memrealtime(); } while (0)

  if (service) {
    const int cg = wg & 15, kq = wg >> 4;
    __builtin_amdgcn_s_setprio(3);
    if (wave < 8) {
      const unsigned cv = *(const GAS unsigned*)(cvec + wave * 512 + kq * 128 + lane * 2);
      tag = __builtin_amdgcn_raw_buffer_load_b32(rs, WS_EPOCH, 0, 16) + 1u;
      const v4u xv = *(const GAS v4u*)(x + wave * 512 + lane * 8);
      const v2u xq = *(const GAS v2u*)(x + wave * 512 + kq * 128 + (lane & 31) * 4);
      *(LAS unsigned*)(L + L_A0 + wave * 272 + lane * 4) = pk(silu(bflo(cv)), silu(bfhi(cv)));
      ctrl[4] = tag;
      BAR();
      if (wave < 2) mod_half(L + L_WM, L + L_A0, rs, WS_MP + kq * 8 * 12288 + cg * 256, wave, tag & TM, lane);
      STAMP(3);
      float v[8], m, inv;
#pragma unroll
      for (int j = 0; j < 4; ++j) { v[2 * j] = bflo(xv[j]); v[2 * j + 1] = bfhi(xv[j]); }
      ln_stats(v, m, inv);
      const float n[4] = {rb(rb(bflo(xq.x) - m) * inv), rb(rb(bfhi(xq.x) - m) * inv), rb(rb(bflo(xq.y) - m) * inv), rb(rb(bfhi(xq.y) - m) * inv)};
      v4u a[4];
#pragma unroll 1
      for (int i = 0; i < HDELAY; ++i) __builtin_amdgcn_s_sleep(8);
      STAMP(1);
      poll_masked<4, 8 * 12288>(rs, WS_MP + wave * 12288 + ((lane >> 5) * 512 + kq * 128 + (lane & 31) * 4) * 4, tag & TM, TM, a);
      STAMP(2);
      const f32x4 sm = (asf(a[0]) + asf(a[1])) + (asf(a[2]) + asf(a[3]));
      float h[4];
#pragma unroll
      for (int j = 0; j < 4; ++j) {
        const auto sw = __builtin_amdgcn_permlane32_swap(__float_as_uint(sm[j]), __float_as_uint(sm[j]), false, false);
        h[j] = rb(n[j] * rb(1.f + rb(__uint_as_float(sw[1])))) + rb(__uint_as_float(sw[0]));
      }
      if (lane < 32) *(LAS v2u*)(L + L_A1 + wave * 272 + lane * 8) = v2u{pk(h[0], h[1]), pk(h[2], h[3])};
      BAR();
      BAR();
    } else {
      const int ct = wave - 8;
      const u16* wm_src = wmod + (size_t)(kq * 128 + ct * 32 + (lane >> 3)) * 3072 + 64 * cg + (((lane & 7) ^ (lane >> 3 & 2)) * 8);
      const u16* wq_src = wq + (size_t)(kq * 128 + ct * 32 + (lane >> 5)) * 4096 + 256 * cg + (((lane & 31) ^ (lane >> 4 & 2)) * 8);
      glds_run(wm_src, 8 * 3072, lbase + L_WM + ct * 4 * 1088, 1088, 4);
      glds_run(wq_src, 2 * 4096, lbase + L_WW + ct * 4 * (4 * 1088 + 128), 1088, 4);
      VM_WAIT(4);
      STAMP(1);
      BAR();
#pragma unroll 1
      for (int j = 1; j < 4; ++j) { glds_run(wq_src + (size_t)j * 8 * 4096, 2 * 4096, lbase + L_WW + (ct * 4 + j) * (4 * 1088 + 128), 1088, 4); VM_WAIT(4); }
      glds_run(wm_src + 1024, 8 * 3072, lbase + L_WR + ct * 4 * 1088, 1088, 4);
      VM_WAIT(4);
      if (lane == 0) __hip_atomic_fetch_add((LAS unsigned*)(L + L_CTRL) + 5, 1u, __ATOMIC_RELAXED, __HIP_MEMORY_SCOPE_WORKGROUP);
      glds_run(wm_src + 2048, 8 * 3072, lbase + L_WR + (16 + ct * 4) * 1088, 1088, 4);
      tag = ctrl[4];
      lds_spin(ctrl + 5, 4u);
      STAMP(3);
      v4u wf[4][4], bf[4];
      {
        const int q = (lane & 15) >> 2;
#pragma unroll
        for (int t = 0; t < 4; ++t) {
          LAS unsigned char* p = L + L_WW + (lane >> 4) * (4 * 1088 + 128) + (q >> 1) * 1088 + (q & 1) * 512 + (((4 * ct + t) ^ (q & 1)) * 32) + 8 * (lane & 3);
#pragma unroll
          for (int st = 0; st < 4; ++st) wf[t][st] = trread2(p + st * (16 * 1088 + 512), p + st * (16 * 1088 + 512) + 2 * 1088);
        }
      }
      BAR();
#pragma unroll
      for (int st = 0; st < 4; ++st) bf[st] = *(LAS v4u*)(L + L_A1 + (lane & 7) * 272 + st * 64 + (lane >> 4) * 16);
      f32x4 a[4];
#pragma unroll
      for (int t = 0; t < 4; ++t) a[t] = f32x4{0.f, 0.f, 0.f, 0.f};
#pragma unroll
      for (int st = 0; st < 4; ++st)
#pragma unroll
        for (int t = 0; t < 4; ++t) a[t] = mfma32(wf[t][st], bf[st], a[t]);
      const int o = WS_QP + (kq * 8 + (lane & 7)) * 16384 + (cg * 256 + ct * 64) * 4 + (lane & 8) * 8 + (lane >> 4) * 16;
      stx(rs, o, tag8r(sel(lane & 8, a[1], a[0]), tag & TM));
      stx(rs, o + 128, tag8r(sel(lane & 8, a[3], a[2]), tag & TM));
      STAMP(4);
      VM_WAIT(2);
      BAR();
      mod_half(L + L_WR + (ct >> 1) * 16 * 1088, L + L_A0, rs, WS_MP + kq * 8 * 12288 + (1024 * ((ct >> 1) + 1) + cg * 64) * 4, ct & 1, tag & TM, lane);
      STAMP(5);
      if (lane == 0) __hip_atomic_fetch_add((LAS unsigned*)(L + L_CTRL) + 5, 1u, __ATOMIC_RELAXED, __HIP_MEMORY_SCOPE_WORKGROUP);
      if (ct == 1) {
#pragma unroll 1
        for (int i = 0; i < 24; ++i) __builtin_amdgcn_s_sleep(8);
        v4u a2[2];
        poll_masked<2, 2 * 8 * 12288>(rs, WS_MP + ((lane >> 5) * 8 + b) * 12288 + (1536 + ((lane >> 4) & 1) * 512 + kvh * 64 + (lane & 15) * 4) * 4, tag & TM, TM, a2);
        const f32x4 sm = asf(a2[0]) + asf(a2[1]);
        unsigned o2[4];
#pragma unroll
        for (int j = 0; j < 4; ++j) {
          const float t2 = x32sum(sm[j]);
          const auto sw = __builtin_amdgcn_permlane16_swap(__float_as_uint(t2), __float_as_uint(t2), false, false);
          o2[j] = pk(__uint_as_float(sw[0]), 1.f + rb(__uint_as_float(sw[1])));
        }
        const bool hi = lane & 16;
        if (lane < 32) stx(rs, WS_SC + (b * 512 + kvh * 64 + (lane & 15) * 4) * 8 + (lane & 16), v4u{hi ? o2[2] : o2[0], tag, hi ? o2[3] : o2[1], tag});
        STAMP(7);
      }
    }
    if (wave != 0) __builtin_amdgcn_s_setprio(0);
  }

  const int natt = service ? 5 : 9;
  const bool roleW = sp == 1 || sp == 2, roleF = !roleW && b < 7;
  const int widx = wg - 64;
  float x1v[4] = {0.f, 0.f, 0.f, 0.f}, gbv[4] = {0.f, 0.f, 0.f, 0.f}, gav[4] = {0.f, 0.f, 0.f, 0.f};

  if (wave == 0) {
    __builtin_amdgcn_s_setprio(3);
    if (!service) { BAR(); tag = __builtin_amdgcn_raw_buffer_load_b32(rs, WS_EPOCH, 0, 16) + 1u; }
    if (!service) {
#pragma unroll 1
      for (int i = 0; i < QDELAY; ++i) __builtin_amdgcn_s_sleep(16);
    }
    v4u q0[4], q1[4];
    const int qoff = WS_QP + b * 16384 + kvh * 2048 + lane * 16;
#pragma unroll 1
    for (unsigned s = 0; s < SPIN_CAP; ++s) {
      MEMBAR();
      unsigned bad = 0;
#pragma unroll
      for (int i = 0; i < 4; ++i) { q0[i] = ldx(rs, qoff + i * 8 * 16384); q1[i] = ldx(rs, qoff + i * 8 * 16384 + 1024); }
#pragma unroll
      for (int i = 0; i < 4; ++i) { q0[i] ^= tag & TM; q1[i] ^= tag & TM; bad |= q0[i].x | q0[i].y | q0[i].z | q0[i].w | q1[i].x | q1[i].y | q1[i].z | q1[i].w; }
      if (__all((bad & TM) == 0)) break;
    }
    const f32x4 qa = (asf(q0[0]) + asf(q0[1])) + (asf(q0[2]) + asf(q0[3])), qb = (asf(q1[0]) + asf(q1[1])) + (asf(q1[2]) + asf(q1[3]));
    *(LAS v2u*)(L + L_Q + (lane >> 5) * 272 + (lane & 31) * 8) = v2u{pk(qa[0], qa[1]), pk(qa[2], qa[3])};
    *(LAS v2u*)(L + L_Q + (2 + (lane >> 5)) * 272 + (lane & 31) * 8) = v2u{pk(qb[0], qb[1]), pk(qb[2], qb[3])};
    ctrl[4] = tag;
    asm volatile("s_waitcnt lgkmcnt(0)" ::: "memory");
    ctrl[0] = 1u;
    DG(1);
    STAMP(6);
    if (roleW) {
      const int ob = widx >> 4, col = (widx & 15) * 32 + (lane & 7) * 4;
      const v2u xv = *(const GAS v2u*)(x + ob * 512 + col);
      v4u g[1];
      __builtin_amdgcn_s_setprio(0);
      lds_spin(ctrl + 1, 9u);
      poll_masked<1, 0>(rs, WS_MP + (((lane >> 3) & 3) * 8 + ob) * 12288 + (1024 + (lane >> 5) * 1536 + col) * 4, tag & TM, TM, g);
#pragma unroll
      for (int j = 0; j < 4; ++j) {
        float t = __uint_as_float(g[0][j]);
        t += dpp<0x128>(t); t = x16sum(t);
        const auto sw = __builtin_amdgcn_permlane32_swap(__float_as_uint(t), __float_as_uint(t), false, false);
        gav[j] = rb(__uint_as_float(sw[0])); gbv[j] = rb(__uint_as_float(sw[1]));
      }
      x1v[0] = bflo(xv.x); x1v[1] = bfhi(xv.x); x1v[2] = bflo(xv.y); x1v[3] = bfhi(xv.y);
    }
    STAMP(7);
  } else if (wave <= natt) {
    const int aw = wave - 1;
    const int tile0 = (service ? 108 : 36 * (sp - 1)) + aw;
    const size_t base = ((size_t)(b * 8 + kvh) * SK + (SK - WIN)) * 256 + (size_t)tile0 * 4096;
    const int r = lane & 15, g = lane >> 4;
    const GAS unsigned char* kp = (const GAS unsigned char*)kc + base + (4 * (r & 3) + (r >> 2)) * 256 + g * 16;
    const GAS unsigned char* vp = (const GAS unsigned char*)vc + base + lane * 16;
    const int tstride = natt * 4096;
    v4u kr[4][4], vr[4][4];
#define LDK(t) _Pragma("unroll") for (int i = 0; i < 4; ++i) kr[t][i] = __builtin_nontemporal_load((const GAS v4u*)(kp + (t) * tstride + i * 64))
#define LDV(t) _Pragma("unroll") for (int i = 0; i < 4; ++i) vr[t][i] = __builtin_nontemporal_load((const GAS v4u*)(vp + (t) * tstride + i * 1024))
    LDK(0); SB();
    if (!service) BAR();
    LDK(1); SB(); LDV(0); SB(); LDK(2); SB(); LDV(1); SB(); LDK(3); SB(); LDV(2); SB(); LDV(3); SB();
    if (lane == 0) __hip_atomic_fetch_add((LAS unsigned*)(L + L_CTRL) + 1, 1u, __ATOMIC_RELAXED, __HIP_MEMORY_SCOPE_WORKGROUP);
    STAMP(8);
    lds_spin(ctrl + 0, 1u);
    f32x4 d[4];
#pragma unroll
    for (int t = 0; t < 4; ++t) d[t] = f32x4{0.f, 0.f, 0.f, 0.f};
#pragma unroll
    for (int st = 0; st < 4; ++st) {
      const v4u qf = *(LAS v4u*)(L + L_Q + (lane & 3) * 272 + st * 64 + g * 16);
#pragma unroll
      for (int t = 0; t < 4; ++t) d[t] = mfma32(kr[t][st], qf, d[t]);
    }
    const f32x4 sv = sel(lane & 8, sel(lane & 4, d[3], d[2]), sel(lane & 4, d[1], d[0]));
    float s[4];
#pragma unroll
    for (int j = 0; j < 4; ++j) s[j] = rb(rb(sv[j]) * 0.08838834764831845f);
    float m = fmaxf(fmaxf(s[0], s[1]), fmaxf(s[2], s[3]));
    m = fmaxf(m, dpp<0x124>(m)); m = fmaxf(m, dpp<0x128>(m)); m = x32max(x16max(m));
    float p[4];
#pragma unroll
    for (int j = 0; j < 4; ++j) p[j] = __builtin_amdgcn_exp2f((s[j] - m) * LOG2E);
    float l = (p[0] + p[1]) + (p[2] + p[3]);
    l += dpp<0x124>(l); l += dpp<0x128>(l); l = x32sum(x16sum(l));
    if (lane < 4) { *(LAS float*)(L + L_ST + lane * 96 + aw * 4) = m; *(LAS float*)(L + L_ST + lane * 96 + 48 + aw * 4) = l; }
    const unsigned p01 = pk(p[0], p[1]), p23 = pk(p[2], p[3]);
    STAMP(9);
    f32x4 acc[8];
#pragma unroll
    for (int dd = 0; dd < 8; ++dd) acc[dd] = f32x4{0.f, 0.f, 0.f, 0.f};
#pragma unroll
    for (int tp = 0; tp < 2; ++tp) {
      const v4u pf = tp == 0 ? v4u{p01, p23, dppu<0x104>(p01), dppu<0x104>(p23)} : v4u{dppu<0x108>(p01), dppu<0x108>(p23), dppu<0x10C>(p01), dppu<0x10C>(p23)};
      v4u vf[8];
#pragma unroll
      for (int u = 0; u < 2; ++u) {
        const int t = 2 * tp + u;
#pragma unroll
        for (int w = 0; w < 4; ++w) {
          vf[2 * w][2 * u] = __builtin_amdgcn_perm(vr[t][1][w], vr[t][0][w], 0x05040100u); vf[2 * w][2 * u + 1] = __builtin_amdgcn_perm(vr[t][3][w], vr[t][2][w], 0x05040100u);
          vf[2 * w + 1][2 * u] = __builtin_amdgcn_perm(vr[t][1][w], vr[t][0][w], 0x07060302u); vf[2 * w + 1][2 * u + 1] = __builtin_amdgcn_perm(vr[t][3][w], vr[t][2][w], 0x07060302u);
        }
        if (u == 0) SB();
      }
#pragma unroll
      for (int dd = 0; dd < 8; ++dd) acc[dd] = mfma32(vf[dd], pf, acc[dd]);
      SB();
    }
    if (r < 4) {
#pragma unroll
      for (int dd = 0; dd < 8; ++dd) *(LAS f32x4*)(L + L_PART + (aw * 4 + r) * 528 + dd * 64 + g * 16) = acc[dd];
    }
    MEMBAR();
    if (lane == 0) __hip_atomic_fetch_add((LAS unsigned*)(L + L_CTRL) + 3, 1u, __ATOMIC_RELAXED, __HIP_MEMORY_SCOPE_WORKGROUP);
    STAMP(10);
  } else {
    if (!service) BAR();
  }
  if (wave == 9) EXIT();
  if (wave >= 10) {
    lds_spin(ctrl + 1, (unsigned)natt);
    __builtin_amdgcn_s_setprio(3);
#pragma unroll 1
    for (unsigned s = 0; s < 64u * SPIN_CAP; ++s) if (ctrl[3] >= (unsigned)natt) break;
    MEMBAR();
    STAMP(11);
    tag = ctrl[4];
    const unsigned t8 = tag & TM;
    const int h = (wave - 10) * 2 + (lane >> 5), p4 = lane & 31;
    const LAS f32x4* st = (const LAS f32x4*)(L + L_ST + h * 96);
    const f32x4 m0 = st[0], m1 = st[1], m2 = st[2], l0 = st[3], l1 = st[4], l2 = st[5];
    f32x4 a[9];
#pragma unroll
    for (int i = 0; i < 9; ++i) a[i] = *(LAS f32x4*)(L + L_PART + (i * 4 + h) * 528 + p4 * 16);
    float mw[9] = {m0[0], m0[1], m0[2], m0[3], m1[0], m1[1], m1[2], m1[3], m2[0]};
    const float lw[9] = {l0[0], l0[1], l0[2], l0[3], l1[0], l1[1], l1[2], l1[3], l2[0]};
    float M = fmaxf(fmaxf(fmaxf(mw[0], mw[1]), fmaxf(mw[2], mw[3])), mw[4]);
    if (!service) M = fmaxf(fmaxf(M, fmaxf(mw[5], mw[6])), fmaxf(mw[7], mw[8]));
    f32x4 o = {0.f, 0.f, 0.f, 0.f}; float ls = 0.f;
#pragma unroll
    for (int i = 0; i < 5; ++i) { const float w = __builtin_amdgcn_exp2f((mw[i] - M) * LOG2E); ls = fmaf(w, lw[i], ls); o = __builtin_elementwise_fma(f32x4{w, w, w, w}, a[i], o); }
    if (!service) {
#pragma unroll
      for (int i = 5; i < 9; ++i) { const float w = __builtin_amdgcn_exp2f((mw[i] - M) * LOG2E); ls = fmaf(w, lw[i], ls); o = __builtin_elementwise_fma(f32x4{w, w, w, w}, a[i], o); }
    }
    const int rec = (b * 32 + kvh * 4 + h) * 4 + sp;
    stx(rs, WS_P1O + rec * 512 + p4 * 16, tag8(o, t8));
    if (p4 == 0) stx(rs, WS_P1S + rec * 16, v4u{__float_as_uint(M), tag, __float_as_uint(ls), tag});
    STAMP(12);
    EXIT();
  }
  if (!roleW && !roleF) EXIT();
  tag = ctrl[4];
  const unsigned t8 = tag & TM;
  if (roleW) {
    if (wave >= 1) {
      const int h = widx >> 2, cg = widx & 3, ob = wave - 1;
      const int rec = (ob * 32 + h) * 4 + (lane >> 5);
      v4u oa, oc, sa, sc; v4u wf[2][4];
      lds_spin(ctrl + WGATE, 9u);
      glds_run(wo + (size_t)(h * 128 + 8 * (lane >> 4) + ob) * 512 + cg * 128 + (((lane & 15) ^ (lane >> 3 & 6)) * 8), 32 * 512, lbase + L_WW + ob * 4 * 1088, 1088, 4);
      VM_WAIT(0);
      if (lane == 0) __hip_atomic_fetch_add((LAS unsigned*)(L + L_CTRL) + 2, 1u, __ATOMIC_RELAXED, __HIP_MEMORY_SCOPE_WORKGROUP);
      lds_spin(ctrl + 2, 8u);
      STAMP(12);
      if (ob < 4) {
        const int q = (lane & 15) >> 2;
#pragma unroll
        for (int ct = 0; ct < 2; ++ct) {
          LAS unsigned char* p = L + L_WW + (lane >> 4) * 2176 + q * 256 + (((2 * ob + ct) ^ q) * 32) + 8 * (lane & 3);
#pragma unroll
          for (int st = 0; st < 4; ++st) wf[ct][st] = trread2(p + st * 8704, p + st * 8704 + 1088);
        }
      }
      lds_spin(ctrl + 3, 9u);
#pragma unroll 1
      for (unsigned s = 0; s < SPIN_CAP; ++s) {
        MEMBAR();
        oa = ldx(rs, WS_P1O + rec * 512 + (lane & 31) * 16); oc = ldx(rs, WS_P1O + rec * 512 + 1024 + (lane & 31) * 16);
        sa = ldx(rs, WS_P1S + rec * 16); sc = ldx(rs, WS_P1S + rec * 16 + 32);
        oa ^= t8; oc ^= t8;
        const unsigned bad = ((oa.x | oa.y | oa.z | oa.w | oc.x | oc.y | oc.z | oc.w) & TM) | (sa.y ^ tag) | (sa.w ^ tag) | (sc.y ^ tag) | (sc.w ^ tag);
        if (__all(bad == 0)) break;
        if (PSLEEP) __builtin_amdgcn_s_sleep(PSLEEP);
      }
      STAMP(13);
      const float ma = __uint_as_float(sa.x), mc = __uint_as_float(sc.x);
      const float M = x32max(fmaxf(ma, mc));
      const float wa = __builtin_amdgcn_exp2f((ma - M) * LOG2E), wc = __builtin_amdgcn_exp2f((mc - M) * LOG2E);
      const float inv = __builtin_amdgcn_rcpf(x32sum(fmaf(wa, __uint_as_float(sa.z), wc * __uint_as_float(sc.z))));
      float o[4];
#pragma unroll
      for (int j = 0; j < 4; ++j) o[j] = x32sum(fmaf(wa, __uint_as_float(oa[j]), wc * __uint_as_float(oc[j]))) * inv;
      if (lane < 32) *(LAS v2u*)(L + L_A1 + ob * 272 + lane * 8) = v2u{pk(o[0], o[1]), pk(o[2], o[3])};
      BAR();
      if (ob >= 4) EXIT();
      v4u bf[4];
#pragma unroll
      for (int st = 0; st < 4; ++st) bf[st] = *(LAS v4u*)(L + L_A1 + (lane & 7) * 272 + st * 64 + (lane >> 4) * 16);
      f32x4 a0 = {0.f, 0.f, 0.f, 0.f}, a1 = {0.f, 0.f, 0.f, 0.f};
#pragma unroll
      for (int st = 0; st < 4; ++st) { a0 = mfma32(wf[0][st], bf[st], a0); a1 = mfma32(wf[1][st], bf[st], a1); }
      stx(rs, WS_P2 + (((lane & 7) * 16 + cg * 4 + ob) * 32 + h) * 128 + (lane & 8) * 8 + (lane >> 4) * 16, tag8(sel(lane & 8, a1, a0), t8));
      STAMP(14);
      EXIT();
    }
    if (DIAG == 2) { lds_spin(ctrl + 3, 9u); DG(2); }
    BAR();
    DG(3);
    const int ob = widx >> 4, cg = widx & 15;
    {
      v4u a[4];
      poll_masked<4, 1024>(rs, WS_P2 + widx * 4096 + lane * 16, t8, TM, a);
      DG(4);
      STAMP(13);
      const f32x4 sm = (asf(a[0]) + asf(a[1])) + (asf(a[2]) + asf(a[3]));
      v4u xo;
#pragma unroll
      for (int j = 0; j < 4; ++j) {
        float t = sm[j];
        t += dpp<0x128>(t); t = x32sum(x16sum(t));
        x1v[j] = rb(x1v[j] + rb(gav[j] * rb(t)));
        xo[j] = __float_as_uint(x1v[j]) | (tag & 0xffffu);
      }
      if (lane < 8) stx(rs, WS_X1 + (ob * 512 + cg * 32 + lane * 4) * 4, xo);
      STAMP(14);
    }
    {
      v4u a[7];
      poll_masked<7, 1024>(rs, WS_P3 + widx * 7168 + lane * 16, t8, TM, a);
      STAMP(12);
      const f32x4 sm = ((asf(a[0]) + asf(a[1])) + (asf(a[2]) + asf(a[3]))) + ((asf(a[4]) + asf(a[5])) + asf(a[6]));
      float y[4];
#pragma unroll
      for (int j = 0; j < 4; ++j) {
        float t = sm[j];
        t += dpp<0x128>(t); t = x32sum(x16sum(t));
        y[j] = x1v[j] + rb(gbv[j] * rb(t));
      }
      if (lane < 8) *(GAS v2u*)(out + ob * 512 + cg * 32 + lane * 4) = v2u{pk(y[0], y[1]), pk(y[2], y[3])};
      if (wg == 64 && lane == 0) __builtin_amdgcn_raw_buffer_store_b32(tag, rs, WS_EPOCH, 0, 16);
      DG(5);
      if (wg == 64) { const unsigned long long until = dg0 + 4000ull + 10ull * (dgx - dg0); while (__builtin_amdgcn_s_memrealtime() < until) __builtin_amdgcn_s_sleep(1); }
    }
    EXIT();
  }
  if (roleF && wave >= 1) {
    const int f = kvh * 7 + b, w = wave - 1, row = (service ? 0 : 4) + (w >> 1), half = w & 1;
    if (service) lds_spin(ctrl + 5, 8u);
    lds_spin(ctrl + FGATE, (unsigned)natt);
    glds_run_thin((half ? wup : wgate) + (size_t)((w >> 1) * 128 + (lane >> 2)) * FF + 32 * f + (((lane & 3) ^ (lane >> 4 & 2)) * 8), 16 * FF, lbase + L_WW + w * 12544, 1024, 8);
    glds_run_thin(wdown + (size_t)(32 * f + (lane >> 3)) * 512 + 64 * w + (((lane & 7) ^ (lane >> 3 & 2)) * 8), 8 * 512, lbase + L_WW + w * 12544 + 8192, 1088, 4);
    v4u sc0, sc1;
#pragma unroll 1
    for (unsigned s = 0; s < SPIN_CAP; ++s) {
      MEMBAR();
      sc0 = ldx(rs, WS_SC + (row * 512 + half * 256 + lane * 4) * 8); sc1 = ldx(rs, WS_SC + (row * 512 + half * 256 + lane * 4) * 8 + 16);
      if (__all(((sc0.y ^ tag) | (sc0.w ^ tag) | (sc1.y ^ tag) | (sc1.w ^ tag)) == 0)) break;
      __builtin_amdgcn_s_sleep(8);
    }
    VM_WAIT(0);
    v4u wf[2][4], wd[4];
    {
      const int q = (lane & 15) >> 2, g = lane >> 4;
#pragma unroll
      for (int ct = 0; ct < 2; ++ct) {
        LAS unsigned char* p = L + L_WW + w * 12544 + (8 * g + q) * 64 + ((ct ^ (g & 1)) * 32) + 8 * (lane & 3);
#pragma unroll
        for (int st = 0; st < 4; ++st) wf[ct][st] = trread2(p + st * 2048, p + st * 2048 + 256);
      }
#pragma unroll
      for (int u = 0; u < 4; ++u) {
        LAS unsigned char* pd = L + L_WW + w * 12544 + 8192 + (g >> 1) * 1088 + ((g & 1) * 4 + q) * 128 + ((u ^ (q >> 1)) * 32) + 8 * (lane & 3);
        const v4u d = trread2(pd, pd + 2 * 1088);
        wd[u] = v4u{d[u], d[(u + 1) & 3], d[(u + 2) & 3], d[(u + 3) & 3]};
      }
    }
    lds_spin(ctrl + 3, (unsigned)natt);
    STAMP(12);
    v4u xr[2];
#pragma unroll 1
    for (unsigned s = 0; s < SPIN_CAP; ++s) {
      MEMBAR();
      xr[0] = ldx(rs, WS_X1 + row * 2048 + half * 1024 + lane * 16); xr[1] = ldx(rs, WS_X1 + row * 2048 + (half ^ 1) * 1024 + lane * 16);
      xr[0] ^= tag & 0xffffu; xr[1] ^= tag & 0xffffu;
      if (__all(((xr[0].x | xr[0].y | xr[0].z | xr[0].w | xr[1].x | xr[1].y | xr[1].z | xr[1].w) & 0xffffu) == 0)) break;
    }
    STAMP(13);
    float v[8], m, inv;
#pragma unroll
    for (int j = 0; j < 4; ++j) { v[j] = __uint_as_float(xr[0][j]); v[4 + j] = __uint_as_float(xr[1][j]); }
    ln_stats(v, m, inv);
    const unsigned scv[4] = {sc0.x, sc0.z, sc1.x, sc1.z};
    float h[4];
#pragma unroll
    for (int j = 0; j < 4; ++j) h[j] = rb(rb(rb(v[j] - m) * inv) * bfhi(scv[j])) + bflo(scv[j]);
    *(LAS v2u*)(L + L_A1 + (w >> 1) * 1040 + half * 512 + lane * 8) = v2u{pk(h[0], h[1]), pk(h[2], h[3])};
    BAR();
    v4u bf[4];
#pragma unroll
    for (int st = 0; st < 4; ++st) bf[st] = *(LAS v4u*)(L + L_A1 + (lane & 3) * 1040 + (w >> 1) * 256 + st * 64 + (lane >> 4) * 16);
    f32x4 a0 = {0.f, 0.f, 0.f, 0.f}, a1 = {0.f, 0.f, 0.f, 0.f};
#pragma unroll
    for (int st = 0; st < 4; ++st) { a0 = mfma32(wf[0][st], bf[st], a0); a1 = mfma32(wf[1][st], bf[st], a1); }
    const f32x4 as = sel(lane & 8, a1, a0);
    *(LAS f32x2*)(L + L_RED + w * 512 + lane * 8) = (lane & 4) ? f32x2{as[2], as[3]} : f32x2{as[0], as[1]};
    BAR();
    LAS unsigned char* pr = L + L_RED + lane * 8;
    const f32x2 gs = (*(LAS f32x2*)(pr) + *(LAS f32x2*)(pr + 1024)) + (*(LAS f32x2*)(pr + 2048) + *(LAS f32x2*)(pr + 3072));
    const f32x2 us = (*(LAS f32x2*)(pr + 512) + *(LAS f32x2*)(pr + 1536)) + (*(LAS f32x2*)(pr + 2560) + *(LAS f32x2*)(pr + 3584));
    const unsigned ax = pk(rb(silu(rb(gs[0]))) * rb(us[0]), rb(silu(rb(gs[1]))) * rb(us[1]));
    const v4u af = {ax, dppu<0x12C>(ax), dppu<0x128>(ax), dppu<0x124>(ax)};
    f32x4 y[4];
#pragma unroll
    for (int u = 0; u < 4; ++u) y[u] = mfma32(wd[u], af, f32x4{0.f, 0.f, 0.f, 0.f});
    const f32x4 ys = sel(lane & 8, sel(lane & 4, y[3], y[2]), sel(lane & 4, y[1], y[0]));
    stx(rs, WS_P3 + ((((service ? 0 : 4) + (lane & 3)) * 16 + 2 * w + ((lane >> 3) & 1)) * 56 + f) * 128 + (lane & 4) * 16 + (lane >> 4) * 16, tag8(ys, t8));
    STAMP(14);
  }
  EXIT();
}

extern "C" void kernel_launch(void* const* d_in, const int* in_sizes, int n_in, void* d_out, int out_size, void* d_ws, size_t ws_size, hipStream_t stream) {
  (void)in_sizes; (void)n_in; (void)out_size; (void)ws_size;
  (void)hipFuncSetAttribute((const void*)fused_block, hipFuncAttributeMaxDynamicSharedMemorySize, LDS_ALLOC);
  fused_block<<<256, NTHREADS, LDS_ALLOC, stream>>>((const u16*)d_in[0], (const u16*)d_in[1], (const u16*)d_in[2], (const u16*)d_in[3], (const u16*)d_in[4],
                                                    (const u16*)d_in[6], (const u16*)d_in[7], (const u16*)d_in[8], (const u16*)d_in[9], (const u16*)d_in[10],
                                                    (u16*)d_out, (unsigned char*)d_ws);
}
```

```cpp
#include <hip/hip_runtime.h>
#pragma clang fp contract(off)
#define QDELAY 8
#define HDELAY 4
#define FGATE 3
#define WGATE 1
#define PSLEEP 0
#define TAILGATE 0

typedef unsigned short u16;
typedef unsigned v4u __attribute__((ext_vector_type(4)));
typedef unsigned v2u __attribute__((ext_vector_type(2)));
typedef float f32x4 __attribute__((ext_vector_type(4)));
typedef float f32x2 __attribute__((ext_vector_type(2)));
typedef short s16x4 __attribute__((ext_vector_type(4)));
typedef __bf16 bf16x8 __attribute__((ext_vector_type(8)));
typedef __bf16 bf16x2 __attribute__((ext_vector_type(2)));
#define GAS __attribute__((address_space(1)))
#define LAS __attribute__((address_space(3)))
#define DI __device__ __forceinline__

constexpr int SK = 8192, WIN = 2048, FF = 1792;
constexpr int NTHREADS = 768;

constexpr int WS_EPOCH = 0;
constexpr int WS_MP = 4096;
constexpr int WS_QP = WS_MP + 4 * 8 * 3072 * 4;
constexpr int WS_SC = WS_QP + 4 * 8 * 4096 * 4;
constexpr int WS_P1S = WS_SC + 8 * 512 * 8;
constexpr int WS_P1O = WS_P1S + 8 * 32 * 4 * 16;
constexpr int WS_P2 = WS_P1O + 8 * 32 * 4 * 512;
constexpr int WS_X1 = WS_P2 + 8 * 16 * 32 * 128;
constexpr int WS_P3 = WS_X1 + 8 * 512 * 4;
constexpr int WS_END = WS_P3 + 8 * 16 * 56 * 128;
constexpr int WS_STAMP = 8 * 1024 * 1024;

constexpr int L_CTRL = 0;
constexpr int L_Q = 64;
constexpr int L_ST = L_Q + 4 * 272;
constexpr int L_PART = L_ST + 4 * 2 * 12 * 4;
constexpr int L_A0 = L_PART + 9 * 4 * 528;
constexpr int L_A1 = L_A0 + 8 * 272;
constexpr int L_RED = L_A1 + 4 * 1040;
constexpr int L_WM = L_RED + 8 * 1024;
constexpr int L_WW = L_WM + 16 * 1088;
constexpr int WQ_BYTES = 64 * 1088 + 16 * 128;
constexpr int L_WR = L_WW + WQ_BYTES;
constexpr int LDS_BYTES = L_WR + 2 * 16 * 1088;
static_assert(L_WW + 8 * 12544 <= LDS_BYTES, "tail weights");
static_assert(LDS_BYTES <= 160 * 1024 && (L_PART % 16) == 0 && (L_A0 % 16) == 0 && (L_WM % 16) == 0 && (L_WW % 16) == 0 && (L_RED % 16) == 0, "lds");

constexpr unsigned SPIN_CAP = 400000u;
constexpr float LOG2E = 1.4426950408889634f;

DI float bflo(unsigned u) { return __uint_as_float(u << 16); }
DI float bfhi(unsigned u) { return __uint_as_float(u & 0xffff0000u); }
DI unsigned pk(float lo, float hi) { bf16x2 v = {(__bf16)lo, (__bf16)hi}; return __builtin_bit_cast(unsigned, v); }
DI float rb(float x) { return (float)(__bf16)x; }
DI float silu(float v) { return v * __builtin_amdgcn_rcpf(1.f + __builtin_amdgcn_exp2f(-LOG2E * v)); }
template <int CTRL> DI float dpp(float x) { return __builtin_bit_cast(float, __builtin_amdgcn_mov_dpp(__builtin_bit_cast(int, x), CTRL, 0xf, 0xf, true)); }
template <int CTRL> DI unsigned dppu(unsigned x) { return (unsigned)__builtin_amdgcn_mov_dpp((int)x, CTRL, 0xf, 0xf, true); }
DI float x16sum(float x) { auto s = __builtin_amdgcn_permlane16_swap(__float_as_uint(x), __float_as_uint(x), false, false); return __uint_as_float(s[0]) + __uint_as_float(s[1]); }
DI float x32sum(float x) { auto s = __builtin_amdgcn_permlane32_swap(__float_as_uint(x), __float_as_uint(x), false, false); return __uint_as_float(s[0]) + __uint_as_float(s[1]); }
DI float x16max(float x) { auto s = __builtin_amdgcn_permlane16_swap(__float_as_uint(x), __float_as_uint(x), false, false); return fmaxf(__uint_as_float(s[0]), __uint_as_float(s[1])); }
DI float x32max(float x) { auto s = __builtin_amdgcn_permlane32_swap(__float_as_uint(x), __float_as_uint(x), false, false); return fmaxf(__uint_as_float(s[0]), __uint_as_float(s[1])); }
DI float wavesum(float x) { x += dpp<0xB1>(x); x += dpp<0x4E>(x); x += dpp<0x141>(x); x += dpp<0x128>(x); return x32sum(x16sum(x)); }
DI f32x4 mfma32(v4u a, v4u b, f32x4 c) { return __builtin_amdgcn_mfma_f32_16x16x32_bf16(__builtin_bit_cast(bf16x8, a), __builtin_bit_cast(bf16x8, b), c, 0, 0, 0); }
DI f32x4 mfma16(v2u a, v2u b, f32x4 c) { return __builtin_amdgcn_mfma_f32_16x16x16bf16_1k(__builtin_bit_cast(s16x4, a), __builtin_bit_cast(s16x4, b), c, 0, 0, 0); }
DI v2u trread(LAS unsigned char* p) { return __builtin_bit_cast(v2u, __builtin_amdgcn_ds_read_tr16_b64_v4i16((LAS s16x4*)p)); }
DI v4u trread2(LAS unsigned char* p0, LAS unsigned char* p1) { const v2u lo = trread(p0), hi = trread(p1); return v4u{lo.x, lo.y, hi.x, hi.y}; }
DI void glds(const void* gsrc, unsigned lds_dst) {
  unsigned keep;
  asm volatile("s_mov_b32 %0, m0\n\ts_mov_b32 m0, %2\n\ts_nop 0\n\tglobal_load_lds_dwordx4 %1, off nt\n\ts_mov_b32 m0, %0" : "=&s"(keep) : "v"(gsrc), "s"(lds_dst) : "memory");
}
DI void glds_run(const u16* src, int stride, unsigned dst, int dstride, int n) {
#pragma unroll 1
  for (int i = 0; i < n; ++i) { glds(src, dst); src += stride; dst += dstride; }
}
DI void glds_run_thin(const u16* src, int stride, unsigned dst, int dstride, int n) {
#pragma unroll 1
  for (int i = 0; i < n; ++i) { glds(src, dst); src += stride; dst += dstride; asm volatile("s_waitcnt vmcnt(1)" ::: "memory"); }
}
#define VM_WAIT(n) asm volatile("s_waitcnt vmcnt(" #n ")" ::: "memory")
#define BAR() do { asm volatile("s_waitcnt lgkmcnt(0)" ::: "memory"); __builtin_amdgcn_s_barrier(); asm volatile("" ::: "memory"); } while (0)
#define MEMBAR() asm volatile("" ::: "memory")
#define SB() __builtin_amdgcn_sched_barrier(0)
typedef __amdgpu_buffer_rsrc_t rsrc_t;
DI v4u ldx(rsrc_t r, int off) { return __builtin_amdgcn_raw_buffer_load_b128(r, off, 0, 16); }
DI void stx(rsrc_t r, int off, v4u v) { __builtin_amdgcn_raw_buffer_store_b128(v, r, off, 0, 16); }
constexpr unsigned TM = 0xfu;
DI v4u tag8(f32x4 v, unsigned t8) {
  v4u u = __builtin_bit_cast(v4u, v);
  return v4u{(u.x & ~TM) | t8, (u.y & ~TM) | t8, (u.z & ~TM) | t8, (u.w & ~TM) | t8};
}
DI v4u tag8r(f32x4 v, unsigned t8) { return tag8(__builtin_bit_cast(f32x4, __builtin_bit_cast(v4u, v) + 8u), t8); }
DI f32x4 asf(v4u u) { return __builtin_bit_cast(f32x4, u); }
DI f32x4 sel(bool c, f32x4 a, f32x4 b) { return f32x4{c ? a[0] : b[0], c ? a[1] : b[1], c ? a[2] : b[2], c ? a[3] : b[3]}; }
DI void lds_spin(volatile LAS unsigned* w, unsigned want) {
#pragma unroll 1
  for (unsigned s = 0; s < 16u * SPIN_CAP; ++s) { if (*w >= want) break; __builtin_amdgcn_s_sleep(1); }
  MEMBAR();
}
template <int N, int STR> DI void poll_masked(rsrc_t rs, int off, unsigned tg, unsigned mask, v4u (&a)[N]) {
#pragma unroll 1
  for (unsigned s = 0; s < SPIN_CAP; ++s) {
    MEMBAR();
    unsigned bad = 0;
#pragma unroll
    for (int i = 0; i < N; ++i) a[i] = ldx(rs, off + i * STR);
#pragma unroll
    for (int i = 0; i < N; ++i) { a[i] ^= tg; bad |= a[i].x | a[i].y | a[i].z | a[i].w; }
    if (__all((bad & mask) == 0)) break;
    if (PSLEEP) __builtin_amdgcn_s_sleep(PSLEEP);
  }
}
#define STAMP(i) do {} while (0)
#define STAMP_INIT() do {} while (0)
#define EXIT() return
constexpr int LDS_ALLOC = LDS_BYTES;

DI void ln_stats(const float (&v)[8], float& m, float& inv) {
  const float mean = wavesum(((v[0] + v[1]) + (v[2] + v[3])) + ((v[4] + v[5]) + (v[6] + v[7]))) * (1.f / 512.f);
  float q0 = 0.f, q1 = 0.f;
#pragma unroll
  for (int j = 0; j < 4; ++j) { const float d0 = v[j] - mean, d1 = v[4 + j] - mean; q0 = fmaf(d0, d0, q0); q1 = fmaf(d1, d1, q1); }
  const float var = rb(wavesum(q0 + q1) * (1.f / 512.f));
  m = rb(mean);
  inv = __builtin_amdgcn_rcpf(rb(__builtin_amdgcn_sqrtf(rb(var + 1e-5f))));
}
DI void mod_half(LAS unsigned char* img, LAS unsigned char* act, rsrc_t rs, int off, int hf, unsigned t8, int lane) {
  const int q = (lane & 15) >> 2;
  v4u wf[2][4], bf[4];
#pragma unroll
  for (int st = 0; st < 4; ++st) bf[st] = *(LAS v4u*)(act + (lane & 7) * 272 + st * 64 + (lane >> 4) * 16);
#pragma unroll
  for (int ct = 0; ct < 2; ++ct) {
    LAS unsigned char* p = img + (lane >> 4) * 1088 + q * 128 + hf * 64 + ((ct ^ (q >> 1)) * 32) + 8 * (lane & 3);
#pragma unroll
    for (int st = 0; st < 4; ++st) wf[ct][st] = trread2(p + st * 4352, p + st * 4352 + 512);
  }
  SB();
  f32x4 a0 = {0.f, 0.f, 0.f, 0.f}, a1 = {0.f, 0.f, 0.f, 0.f};
#pragma unroll
  for (int st = 0; st < 4; ++st) { a0 = mfma32(wf[0][st], bf[st], a0); a1 = mfma32(wf[1][st], bf[st], a1); }
  stx(rs, off + hf * 128 + (lane & 7) * 12288 + (lane & 8) * 8 + (lane >> 4) * 16, tag8r(sel(lane & 8, a1, a0), t8));
}

__global__ void __launch_bounds__(NTHREADS, 3)
fused_block(const u16* __restrict__ x, const u16* __restrict__ kc, const u16* __restrict__ vc, const u16* __restrict__ wq, const u16* __restrict__ wo,
            const u16* __restrict__ wgate, const u16* __restrict__ wup, const u16* __restrict__ wdown, const u16* __restrict__ cvec, const u16* __restrict__ wmod,
            u16* __restrict__ out, unsigned char* ws) {
  extern __shared__ __attribute__((aligned(16))) unsigned char smem[];
  LAS unsigned char* L = (LAS unsigned char*)smem;
  volatile LAS unsigned* ctrl = (volatile LAS unsigned*)(L + L_CTRL);
  const int wg = blockIdx.x, lane = threadIdx.x & 63, wave = __builtin_amdgcn_readfirstlane(threadIdx.x >> 6);
  const int kvh = wg & 7, b = (wg >> 3) & 7, sp = wg >> 6;
  const bool service = sp == 0;
  const rsrc_t rs = __builtin_amdgcn_make_buffer_rsrc(ws, 0, WS_END, 0x00020000);
  const unsigned lbase = (unsigned)(size_t)L;
  STAMP_INIT();
  STAMP(0);
  if (threadIdx.x < 8 && threadIdx.x != 4) ctrl[threadIdx.x] = 0u;
  unsigned tag = 0;

  if (service) {
    const int cg = wg & 15, kq = wg >> 4;
    __builtin_amdgcn_s_setprio(3);
    if (wave < 8) {
      const unsigned cv = *(const GAS unsigned*)(cvec + wave * 512 + kq * 128 + lane * 2);
      tag = __builtin_amdgcn_raw_buffer_load_b32(rs, WS_EPOCH, 0, 16) + 1u;
      const v4u xv = *(const GAS v4u*)(x + wave * 512 + lane * 8);
      const v2u xq = *(const GAS v2u*)(x + wave * 512 + kq * 128 + (lane & 31) * 4);
      *(LAS unsigned*)(L + L_A0 + wave * 272 + lane * 4) = pk(silu(bflo(cv)), silu(bfhi(cv)));
      ctrl[4] = tag;
      BAR();
      if (wave < 2) mod_half(L + L_WM, L + L_A0, rs, WS_MP + kq * 8 * 12288 + cg * 256, wave, tag & TM, lane);
      STAMP(3);
      float v[8], m, inv;
#pragma unroll
      for (int j = 0; j < 4; ++j) { v[2 * j] = bflo(xv[j]); v[2 * j + 1] = bfhi(xv[j]); }
      ln_stats(v, m, inv);
      const float n[4] = {rb(rb(bflo(xq.x) - m) * inv), rb(rb(bfhi(xq.x) - m) * inv), rb(rb(bflo(xq.y) - m) * inv), rb(rb(bfhi(xq.y) - m) * inv)};
      v4u a[4];
#pragma unroll 1
      for (int i = 0; i < HDELAY; ++i) __builtin_amdgcn_s_sleep(8);
      STAMP(1);
      poll_masked<4, 8 * 12288>(rs, WS_MP + wave * 12288 + ((lane >> 5) * 512 + kq * 128 + (lane & 31) * 4) * 4, tag & TM, TM, a);
      STAMP(2);
      const f32x4 sm = (asf(a[0]) + asf(a[1])) + (asf(a[2]) + asf(a[3]));
      float h[4];
#pragma unroll
      for (int j = 0; j < 4; ++j) {
        const auto sw = __builtin_amdgcn_permlane32_swap(__float_as_uint(sm[j]), __float_as_uint(sm[j]), false, false);
        h[j] = rb(n[j] * rb(1.f + rb(__uint_as_float(sw[1])))) + rb(__uint_as_float(sw[0]));
      }
      if (lane < 32) *(LAS v2u*)(L + L_A1 + wave * 272 + lane * 8) = v2u{pk(h[0], h[1]), pk(h[2], h[3])};
      BAR();
      BAR();
    } else {
      const int ct = wave - 8;
      const u16* wm_src = wmod + (size_t)(kq * 128 + ct * 32 + (lane >> 3)) * 3072 + 64 * cg + (((lane & 7) ^ (lane >> 3 & 2)) * 8);
      const u16* wq_src = wq + (size_t)(kq * 128 + ct * 32 + (lane >> 5)) * 4096 + 256 * cg + (((lane & 31) ^ (lane >> 4 & 2)) * 8);
      glds_run(wm_src, 8 * 3072, lbase + L_WM + ct * 4 * 1088, 1088, 4);
      glds_run(wq_src, 2 * 4096, lbase + L_WW + ct * 4 * (4 * 1088 + 128), 1088, 4);
      VM_WAIT(4);
      STAMP(1);
      BAR();
#pragma unroll 1
      for (int j = 1; j < 4; ++j) { glds_run(wq_src + (size_t)j * 8 * 4096, 2 * 4096, lbase + L_WW + (ct * 4 + j) * (4 * 1088 + 128), 1088, 4); VM_WAIT(4); }
      glds_run(wm_src + 1024, 8 * 3072, lbase + L_WR + ct * 4 * 1088, 1088, 4);
      VM_WAIT(4);
      if (lane == 0) __hip_atomic_fetch_add((LAS unsigned*)(L + L_CTRL) + 5, 1u, __ATOMIC_RELAXED, __HIP_MEMORY_SCOPE_WORKGROUP);
      glds_run(wm_src + 2048, 8 * 3072, lbase + L_WR + (16 + ct * 4) * 1088, 1088, 4);
      tag = ctrl[4];
      lds_spin(ctrl + 5, 4u);
      STAMP(3);
      v4u wf[4][4], bf[4];
      {
        const int q = (lane & 15) >> 2;
#pragma unroll
        for (int t = 0; t < 4; ++t) {
          LAS unsigned char* p = L + L_WW + (lane >> 4) * (4 * 1088 + 128) + (q >> 1) * 1088 + (q & 1) * 512 + (((4 * ct + t) ^ (q & 1)) * 32) + 8 * (lane & 3);
#pragma unroll
          for (int st = 0; st < 4; ++st) wf[t][st] = trread2(p + st * (16 * 1088 + 512), p + st * (16 * 1088 + 512) + 2 * 1088);
        }
      }
      BAR();
#pragma unroll
      for (int st = 0; st < 4; ++st) bf[st] = *(LAS v4u*)(L + L_A1 + (lane & 7) * 272 + st * 64 + (lane >> 4) * 16);
      f32x4 a[4];
#pragma unroll
      for (int t = 0; t < 4; ++t) a[t] = f32x4{0.f, 0.f, 0.f, 0.f};
#pragma unroll
      for (int st = 0; st < 4; ++st)
#pragma unroll
        for (int t = 0; t < 4; ++t) a[t] = mfma32(wf[t][st], bf[st], a[t]);
      const int o = WS_QP + (kq * 8 + (lane & 7)) * 16384 + (cg * 256 + ct * 64) * 4 + (lane & 8) * 8 + (lane >> 4) * 16;
      stx(rs, o, tag8r(sel(lane & 8, a[1], a[0]), tag & TM));
      stx(rs, o + 128, tag8r(sel(lane & 8, a[3], a[2]), tag & TM));
      STAMP(4);
      VM_WAIT(2);
      BAR();
      mod_half(L + L_WR + (ct >> 1) * 16 * 1088, L + L_A0, rs, WS_MP + kq * 8 * 12288 + (1024 * ((ct >> 1) + 1) + cg * 64) * 4, ct & 1, tag & TM, lane);
      STAMP(5);
      if (lane == 0) __hip_atomic_fetch_add((LAS unsigned*)(L + L_CTRL) + 5, 1u, __ATOMIC_RELAXED, __HIP_MEMORY_SCOPE_WORKGROUP);
      if (ct == 1) {
#pragma unroll 1
        for (int i = 0; i < 24; ++i) __builtin_amdgcn_s_sleep(8);
        v4u a2[2];
        poll_masked<2, 2 * 8 * 12288>(rs, WS_MP + ((lane >> 5) * 8 + b) * 12288 + (1536 + ((lane >> 4) & 1) * 512 + kvh * 64 + (lane & 15) * 4) * 4, tag & TM, TM, a2);
        const f32x4 sm = asf(a2[0]) + asf(a2[1]);
        unsigned o2[4];
#pragma unroll
        for (int j = 0; j < 4; ++j) {
          const float t2 = x32sum(sm[j]);
          const auto sw = __builtin_amdgcn_permlane16_swap(__float_as_uint(t2), __float_as_uint(t2), false, false);
          o2[j] = pk(__uint_as_float(sw[0]), 1.f + rb(__uint_as_float(sw[1])));
        }
        const bool hi = lane & 16;
        if (lane < 32) stx(rs, WS_SC + (b * 512 + kvh * 64 + (lane & 15) * 4) * 8 + (lane & 16), v4u{hi ? o2[2] : o2[0], tag, hi ? o2[3] : o2[1], tag});
        STAMP(7);
      }
    }
    if (wave != 0) __builtin_amdgcn_s_setprio(0);
  }

  const int natt = service ? 5 : 9;
  const bool roleW = sp == 1 || sp == 2, roleF = !roleW && b < 7;
  const int widx = wg - 64;
  float x1v[4] = {0.f, 0.f, 0.f, 0.f}, gbv[4] = {0.f, 0.f, 0.f, 0.f}, gav[4] = {0.f, 0.f, 0.f, 0.f};

  if (wave == 0) {
    __builtin_amdgcn_s_setprio(3);
    if (!service) { BAR(); tag = __builtin_amdgcn_raw_buffer_load_b32(rs, WS_EPOCH, 0, 16) + 1u; }
    if (!service) {
#pragma unroll 1
      for (int i = 0; i < QDELAY; ++i) __builtin_amdgcn_s_sleep(16);
    }
    v4u q0[4], q1[4];
    const int qoff = WS_QP + b * 16384 + kvh * 2048 + lane * 16;
#pragma unroll 1
    for (unsigned s = 0; s < SPIN_CAP; ++s) {
      MEMBAR();
      unsigned bad = 0;
#pragma unroll
      for (int i = 0; i < 4; ++i) { q0[i] = ldx(rs, qoff + i * 8 * 16384); q1[i] = ldx(rs, qoff + i * 8 * 16384 + 1024); }
#pragma unroll
      for (int i = 0; i < 4; ++i) { q0[i] ^= tag & TM; q1[i] ^= tag & TM; bad |= q0[i].x | q0[i].y | q0[i].z | q0[i].w | q1[i].x | q1[i].y | q1[i].z | q1[i].w; }
      if (__all((bad & TM) == 0)) break;
    }
    const f32x4 qa = (asf(q0[0]) + asf(q0[1])) + (asf(q0[2]) + asf(q0[3])), qb = (asf(q1[0]) + asf(q1[1])) + (asf(q1[2]) + asf(q1[3]));
    *(LAS v2u*)(L + L_Q + (lane >> 5) * 272 + (lane & 31) * 8) = v2u{pk(qa[0], qa[1]), pk(qa[2], qa[3])};
    *(LAS v2u*)(L + L_Q + (2 + (lane >> 5)) * 272 + (lane & 31) * 8) = v2u{pk(qb[0], qb[1]), pk(qb[2], qb[3])};
    ctrl[4] = tag;
    asm volatile("s_waitcnt lgkmcnt(0)" ::: "memory");
    ctrl[0] = 1u;
    STAMP(6);
    if (roleW) {
      const int ob = widx >> 4, col = (widx & 15) * 32 + (lane & 7) * 4;
      const v2u xv = *(const GAS v2u*)(x + ob * 512 + col);
      v4u g[1];
      __builtin_amdgcn_s_setprio(0);
      lds_spin(ctrl + 1, 9u);
      poll_masked<1, 0>(rs, WS_MP + (((lane >> 3) & 3) * 8 + ob) * 12288 + (1024 + (lane >> 5) * 1536 + col) * 4, tag & TM, TM, g);
#pragma unroll
      for (int j = 0; j < 4; ++j) {
        float t = __uint_as_float(g[0][j]);
        t += dpp<0x128>(t); t = x16sum(t);
        const auto sw = __builtin_amdgcn_permlane32_swap(__float_as_uint(t), __float_as_uint(t), false, false);
        gav[j] = rb(__uint_as_float(sw[0])); gbv[j] = rb(__uint_as_float(sw[1]));
      }
      x1v[0] = bflo(xv.x); x1v[1] = bfhi(xv.x); x1v[2] = bflo(xv.y); x1v[3] = bfhi(xv.y);
    }
    STAMP(7);
  } else if (wave <= natt) {
    const int aw = wave - 1;
    const int tile0 = (service ? 108 : 36 * (sp - 1)) + aw;
    const size_t base = ((size_t)(b * 8 + kvh) * SK + (SK - WIN)) * 256 + (size_t)tile0 * 4096;
    const int r = lane & 15, g = lane >> 4;
    const GAS unsigned char* kp = (const GAS unsigned char*)kc + base + (4 * (r & 3) + (r >> 2)) * 256 + g * 16;
    const GAS unsigned char* vp = (const GAS unsigned char*)vc + base + lane * 16;
    const int tstride = natt * 4096;
    v4u kr[4][4], vr[4][4];
#define LDK(t) _Pragma("unroll") for (int i = 0; i < 4; ++i) kr[t][i] = __builtin_nontemporal_load((const GAS v4u*)(kp + (t) * tstride + i * 64))
#define LDV(t) _Pragma("unroll") for (int i = 0; i < 4; ++i) vr[t][i] = __builtin_nontemporal_load((const GAS v4u*)(vp + (t) * tstride + i * 1024))
    LDK(0); SB();
    if (!service) BAR();
    LDK(1); SB(); LDV(0); SB(); LDK(2); SB(); LDV(1); SB(); LDK(3); SB(); LDV(2); SB(); LDV(3); SB();
    if (lane == 0) __hip_atomic_fetch_add((LAS unsigned*)(L + L_CTRL) + 1, 1u, __ATOMIC_RELAXED, __HIP_MEMORY_SCOPE_WORKGROUP);
    STAMP(8);
    lds_spin(ctrl + 0, 1u);
    f32x4 d[4];
#pragma unroll
    for (int t = 0; t < 4; ++t) d[t] = f32x4{0.f, 0.f, 0.f, 0.f};
#pragma unroll
    for (int st = 0; st < 4; ++st) {
      const v4u qf = *(LAS v4u*)(L + L_Q + (lane & 3) * 272 + st * 64 + g * 16);
#pragma unroll
      for (int t = 0; t < 4; ++t) d[t] = mfma32(kr[t][st], qf, d[t]);
    }
    const f32x4 sv = sel(lane & 8, sel(lane & 4, d[3], d[2]), sel(lane & 4, d[1], d[0]));
    float s[4];
#pragma unroll
    for (int j = 0; j < 4; ++j) s[j] = rb(rb(sv[j]) * 0.08838834764831845f);
    float m = fmaxf(fmaxf(s[0], s[1]), fmaxf(s[2], s[3]));
    m = fmaxf(m, dpp<0x124>(m)); m = fmaxf(m, dpp<0x128>(m)); m = x32max(x16max(m));
    float p[4];
#pragma unroll
    for (int j = 0; j < 4; ++j) p[j] = __builtin_amdgcn_exp2f((s[j] - m) * LOG2E);
    float l = (p[0] + p[1]) + (p[2] + p[3]);
    l += dpp<0x124>(l); l += dpp<0x128>(l); l = x32sum(x16sum(l));
    if (lane < 4) { *(LAS float*)(L + L_ST + lane * 96 + aw * 4) = m; *(LAS float*)(L + L_ST + lane * 96 + 48 + aw * 4) = l; }
    const unsigned p01 = pk(p[0], p[1]), p23 = pk(p[2], p[3]);
    STAMP(9);
    f32x4 acc[8];
#pragma unroll
    for (int dd = 0; dd < 8; ++dd) acc[dd] = f32x4{0.f, 0.f, 0.f, 0.f};
#pragma unroll
    for (int tp = 0; tp < 2; ++tp) {
      const v4u pf = tp == 0 ? v4u{p01, p23, dppu<0x104>(p01), dppu<0x104>(p23)} : v4u{dppu<0x108>(p01), dppu<0x108>(p23), dppu<0x10C>(p01), dppu<0x10C>(p23)};
      v4u vf[8];
#pragma unroll
      for (int u = 0; u < 2; ++u) {
        const int t = 2 * tp + u;
#pragma unroll
        for (int w = 0; w < 4; ++w) {
          vf[2 * w][2 * u] = __builtin_amdgcn_perm(vr[t][1][w], vr[t][0][w], 0x05040100u); vf[2 * w][2 * u + 1] = __builtin_amdgcn_perm(vr[t][3][w], vr[t][2][w], 0x05040100u);
          vf[2 * w + 1][2 * u] = __builtin_amdgcn_perm(vr[t][1][w], vr[t][0][w], 0x07060302u); vf[2 * w + 1][2 * u + 1] = __builtin_amdgcn_perm(vr[t][3][w], vr[t][2][w], 0x07060302u);
        }
        if (u == 0) SB();
      }
#pragma unroll
      for (int dd = 0; dd < 8; ++dd) acc[dd] = mfma32(vf[dd], pf, acc[dd]);
      SB();
    }
    if (r < 4) {
#pragma unroll
      for (int dd = 0; dd < 8; ++dd) *(LAS f32x4*)(L + L_PART + (aw * 4 + r) * 528 + dd * 64 + g * 16) = acc[dd];
    }
    MEMBAR();
    if (lane == 0) __hip_atomic_fetch_add((LAS unsigned*)(L + L_CTRL) + 3, 1u, __ATOMIC_RELAXED, __HIP_MEMORY_SCOPE_WORKGROUP);
    STAMP(10);
  } else {
    if (!service) BAR();
  }
  if (wave == 9) EXIT();
  if (wave >= 10) {
    lds_spin(ctrl + 1, (unsigned)natt);
    __builtin_amdgcn_s_setprio(3);
#pragma unroll 1
    for (unsigned s = 0; s < 64u * SPIN_CAP; ++s) if (ctrl[3] >= (unsigned)natt) break;
    MEMBAR();
    STAMP(11);
    tag = ctrl[4];
    const unsigned t8 = tag & TM;
    const int h = (wave - 10) * 2 + (lane >> 5), p4 = lane & 31;
    const LAS f32x4* st = (const LAS f32x4*)(L + L_ST + h * 96);
    const f32x4 m0 = st[0], m1 = st[1], m2 = st[2], l0 = st[3], l1 = st[4], l2 = st[5];
    f32x4 a[9];
#pragma unroll
    for (int i = 0; i < 9; ++i) a[i] = *(LAS f32x4*)(L + L_PART + (i * 4 + h) * 528 + p4 * 16);
    float mw[9] = {m0[0], m0[1], m0[2], m0[3], m1[0], m1[1], m1[2], m1[3], m2[0]};
    const float lw[9] = {l0[0], l0[1], l0[2], l0[3], l1[0], l1[1], l1[2], l1[3], l2[0]};
    float M = fmaxf(fmaxf(fmaxf(mw[0], mw[1]), fmaxf(mw[2], mw[3])), mw[4]);
    if (!service) M = fmaxf(fmaxf(M, fmaxf(mw[5], mw[6])), fmaxf(mw[7], mw[8]));
    f32x4 o = {0.f, 0.f, 0.f, 0.f}; float ls = 0.f;
#pragma unroll
    for (int i = 0; i < 5; ++i) { const float w = __builtin_amdgcn_exp2f((mw[i] - M) * LOG2E); ls = fmaf(w, lw[i], ls); o = __builtin_elementwise_fma(f32x4{w, w, w, w}, a[i], o); }
    if (!service) {
#pragma unroll
      for (int i = 5; i < 9; ++i) { const float w = __builtin_amdgcn_exp2f((mw[i] - M) * LOG2E); ls = fmaf(w, lw[i], ls); o = __builtin_elementwise_fma(f32x4{w, w, w, w}, a[i], o); }
    }
    const int rec = (b * 32 + kvh * 4 + h) * 4 + sp;
    stx(rs, WS_P1O + rec * 512 + p4 * 16, tag8(o, t8));
    if (p4 == 0) stx(rs, WS_P1S + rec * 16, v4u{__float_as_uint(M), tag, __float_as_uint(ls), tag});
    STAMP(12);
    EXIT();
  }
  if (!roleW && !roleF) EXIT();
  tag = ctrl[4];
  const unsigned t8 = tag & TM;
  if (roleW) {
    if (wave >= 1) {
      const int h = widx >> 2, cg = widx & 3, ob = wave - 1;
      const int rec = (ob * 32 + h) * 4 + (lane >> 5);
      v4u oa, oc, sa, sc; v4u wf[2][4];
      lds_spin(ctrl + WGATE, 9u);
      glds_run(wo + (size_t)(h * 128 + 8 * (lane >> 4) + ob) * 512 + cg * 128 + (((lane & 15) ^ (lane >> 3 & 6)) * 8), 32 * 512, lbase + L_WW + ob * 4 * 1088, 1088, 4);
      VM_WAIT(0);
      if (lane == 0) __hip_atomic_fetch_add((LAS unsigned*)(L + L_CTRL) + 2, 1u, __ATOMIC_RELAXED, __HIP_MEMORY_SCOPE_WORKGROUP);
      lds_spin(ctrl + 2, 8u);
      STAMP(12);
      if (ob < 4) {
        const int q = (lane & 15) >> 2;
#pragma unroll
        for (int ct = 0; ct < 2; ++ct) {
          LAS unsigned char* p = L + L_WW + (lane >> 4) * 2176 + q * 256 + (((2 * ob + ct) ^ q) * 32) + 8 * (lane & 3);
#pragma unroll
          for (int st = 0; st < 4; ++st) wf[ct][st] = trread2(p + st * 8704, p + st * 8704 + 1088);
        }
      }
      lds_spin(ctrl + 3, 9u);
#pragma unroll 1
      for (unsigned s = 0; s < SPIN_CAP; ++s) {
        MEMBAR();
        oa = ldx(rs, WS_P1O + rec * 512 + (lane & 31) * 16); oc = ldx(rs, WS_P1O + rec * 512 + 1024 + (lane & 31) * 16);
        sa = ldx(rs, WS_P1S + rec * 16); sc = ldx(rs, WS_P1S + rec * 16 + 32);
        oa ^= t8; oc ^= t8;
        const unsigned bad = ((oa.x | oa.y | oa.z | oa.w | oc.x | oc.y | oc.z | oc.w) & TM) | (sa.y ^ tag) | (sa.w ^ tag) | (sc.y ^ tag) | (sc.w ^ tag);
        if (__all(bad == 0)) break;
        if (PSLEEP) __builtin_amdgcn_s_sleep(PSLEEP);
      }
      STAMP(13);
      const float ma = __uint_as_float(sa.x), mc = __uint_as_float(sc.x);
      const float M = x32max(fmaxf(ma, mc));
      const float wa = __builtin_amdgcn_exp2f((ma - M) * LOG2E), wc = __builtin_amdgcn_exp2f((mc - M) * LOG2E);
      const float inv = __builtin_amdgcn_rcpf(x32sum(fmaf(wa, __uint_as_float(sa.z), wc * __uint_as_float(sc.z))));
      float o[4];
#pragma unroll
      for (int j = 0; j < 4; ++j) o[j] = x32sum(fmaf(wa, __uint_as_float(oa[j]), wc * __uint_as_float(oc[j]))) * inv;
      if (lane < 32) *(LAS v2u*)(L + L_A1 + ob * 272 + lane * 8) = v2u{pk(o[0], o[1]), pk(o[2], o[3])};
      BAR();
      if (ob >= 4) EXIT();
      v4u bf[4];
#pragma unroll
      for (int st = 0; st < 4; ++st) bf[st] = *(LAS v4u*)(L + L_A1 + (lane & 7) * 272 + st * 64 + (lane >> 4) * 16);
      f32x4 a0 = {0.f, 0.f, 0.f, 0.f}, a1 = {0.f, 0.f, 0.f, 0.f};
#pragma unroll
      for (int st = 0; st < 4; ++st) { a0 = mfma32(wf[0][st], bf[st], a0); a1 = mfma32(wf[1][st], bf[st], a1); }
      stx(rs, WS_P2 + (((lane & 7) * 16 + cg * 4 + ob) * 32 + h) * 128 + (lane & 8) * 8 + (lane >> 4) * 16, tag8(sel(lane & 8, a1, a0), t8));
      STAMP(14);
      EXIT();
    }
    BAR();
    const int ob = widx >> 4, cg = widx & 15;
    {
      v4u a[4];
      poll_masked<4, 1024>(rs, WS_P2 + widx * 4096 + lane * 16, t8, TM, a);
      STAMP(13);
      const f32x4 sm = (asf(a[0]) + asf(a[1])) + (asf(a[2]) + asf(a[3]));
      v4u xo;
#pragma unroll
      for (int j = 0; j < 4; ++j) {
        float t = sm[j];
        t += dpp<0x128>(t); t = x32sum(x16sum(t));
        x1v[j] = rb(x1v[j] + rb(gav[j] * rb(t)));
        xo[j] = __float_as_uint(x1v[j]) | (tag & 0xffffu);
      }
      if (lane < 8) stx(rs, WS_X1 + (ob * 512 + cg * 32 + lane * 4) * 4, xo);
      STAMP(14);
    }
    {
      v4u a[7];
      poll_masked<7, 1024>(rs, WS_P3 + widx * 7168 + lane * 16, t8, TM, a);
      STAMP(12);
      const f32x4 sm = ((asf(a[0]) + asf(a[1])) + (asf(a[2]) + asf(a[3]))) + ((asf(a[4]) + asf(a[5])) + asf(a[6]));
      float y[4];
#pragma unroll
      for (int j = 0; j < 4; ++j) {
        float t = sm[j];
        t += dpp<0x128>(t); t = x32sum(x16sum(t));
        y[j] = x1v[j] + rb(gbv[j] * rb(t));
      }
      if (lane < 8) *(GAS v2u*)(out + ob * 512 + cg * 32 + lane * 4) = v2u{pk(y[0], y[1]), pk(y[2], y[3])};
      if (wg == 64 && lane == 0) __builtin_amdgcn_raw_buffer_store_b32(tag, rs, WS_EPOCH, 0, 16);
      STAMP(15);
    }
    EXIT();
  }
  if (roleF && wave >= 1) {
    const int f = kvh * 7 + b, w = wave - 1, row = (service ? 0 : 4) + (w >> 1), half = w & 1;
    if (service) lds_spin(ctrl + 5, 8u);
    lds_spin(ctrl + FGATE, (unsigned)natt);
    glds_run_thin((half ? wup : wgate) + (size_t)((w >> 1) * 128 + (lane >> 2)) * FF + 32 * f + (((lane & 3) ^ (lane >> 4 & 2)) * 8), 16 * FF, lbase + L_WW + w * 12544, 1024, 8);
    glds_run_thin(wdown + (size_t)(32 * f + (lane >> 3)) * 512 + 64 * w + (((lane & 7) ^ (lane >> 3 & 2)) * 8), 8 * 512, lbase + L_WW + w * 12544 + 8192, 1088, 4);
    v4u sc0, sc1;
#pragma unroll 1
    for (unsigned s = 0; s < SPIN_CAP; ++s) {
      MEMBAR();
      sc0 = ldx(rs, WS_SC + (row * 512 + half * 256 + lane * 4) * 8); sc1 = ldx(rs, WS_SC + (row * 512 + half * 256 + lane * 4) * 8 + 16);
      if (__all(((sc0.y ^ tag) | (sc0.w ^ tag) | (sc1.y ^ tag) | (sc1.w ^ tag)) == 0)) break;
      __builtin_amdgcn_s_sleep(8);
    }
    VM_WAIT(0);
    v4u wf[2][4], wd[4];
    {
      const int q = (lane & 15) >> 2, g = lane >> 4;
#pragma unroll
      for (int ct = 0; ct < 2; ++ct) {
        LAS unsigned char* p = L + L_WW + w * 12544 + (8 * g + q) * 64 + ((ct ^ (g & 1)) * 32) + 8 * (lane & 3);
#pragma unroll
        for (int st = 0; st < 4; ++st) wf[ct][st] = trread2(p + st * 2048, p + st * 2048 + 256);
      }
#pragma unroll
      for (int u = 0; u < 4; ++u) {
        LAS unsigned char* pd = L + L_WW + w * 12544 + 8192 + (g >> 1) * 1088 + ((g & 1) * 4 + q) * 128 + ((u ^ (q >> 1)) * 32) + 8 * (lane & 3);
        const v4u d = trread2(pd, pd + 2 * 1088);
        wd[u] = v4u{d[u], d[(u + 1) & 3], d[(u + 2) & 3], d[(u + 3) & 3]};
      }
    }
    lds_spin(ctrl + 3, (unsigned)natt);
    STAMP(12);
    v4u xr[2];
#pragma unroll 1
    for (unsigned s = 0; s < SPIN_CAP; ++s) {
      MEMBAR();
      xr[0] = ldx(rs, WS_X1 + row * 2048 + half * 1024 + lane * 16); xr[1] = ldx(rs, WS_X1 + row * 2048 + (half ^ 1) * 1024 + lane * 16);
      xr[0] ^= tag & 0xffffu; xr[1] ^= tag & 0xffffu;
      if (__all(((xr[0].x | xr[0].y | xr[0].z | xr[0].w | xr[1].x | xr[1].y | xr[1].z | xr[1].w) & 0xffffu) == 0)) break;
    }
    STAMP(13);
    float v[8], m, inv;
#pragma unroll
    for (int j = 0; j < 4; ++j) { v[j] = __uint_as_float(xr[0][j]); v[4 + j] = __uint_as_float(xr[1][j]); }
    ln_stats(v, m, inv);
    const unsigned scv[4] = {sc0.x, sc0.z, sc1.x, sc1.z};
    float h[4];
#pragma unroll
    for (int j = 0; j < 4; ++j) h[j] = rb(rb(rb(v[j] - m) * inv) * bfhi(scv[j])) + bflo(scv[j]);
    *(LAS v2u*)(L + L_A1 + (w >> 1) * 1040 + half * 512 + lane * 8) = v2u{pk(h[0], h[1]), pk(h[2], h[3])};
    BAR();
    v4u bf[4];
#pragma unroll
    for (int st = 0; st < 4; ++st) bf[st] = *(LAS v4u*)(L + L_A1 + (lane & 3) * 1040 + (w >> 1) * 256 + st * 64 + (lane >> 4) * 16);
    f32x4 a0 = {0.f, 0.f, 0.f, 0.f}, a1 = {0.f, 0.f, 0.f, 0.f};
#pragma unroll
    for (int st = 0; st < 4; ++st) { a0 = mfma32(wf[0][st], bf[st], a0); a1 = mfma32(wf[1][st], bf[st], a1); }
    const f32x4 as = sel(lane & 8, a1, a0);
    *(LAS f32x2*)(L + L_RED + w * 512 + lane * 8) = (lane & 4) ? f32x2{as[2], as[3]} : f32x2{as[0], as[1]};
    BAR();
    LAS unsigned char* pr = L + L_RED + lane * 8;
    const f32x2 gs = (*(LAS f32x2*)(pr) + *(LAS f32x2*)(pr + 1024)) + (*(LAS f32x2*)(pr + 2048) + *(LAS f32x2*)(pr + 3072));
    const f32x2 us = (*(LAS f32x2*)(pr + 512) + *(LAS f32x2*)(pr + 1536)) + (*(LAS f32x2*)(pr + 2560) + *(LAS f32x2*)(pr + 3584));
    const unsigned ax = pk(rb(silu(rb(gs[0]))) * rb(us[0]), rb(silu(rb(gs[1]))) * rb(us[1]));
    const v4u af = {ax, dppu<0x12C>(ax), dppu<0x128>(ax), dppu<0x124>(ax)};
    f32x4 y[4];
#pragma unroll
    for (int u = 0; u < 4; ++u) y[u] = mfma32(wd[u], af, f32x4{0.f, 0.f, 0.f, 0.f});
    const f32x4 ys = sel(lane & 8, sel(lane & 4, y[3], y[2]), sel(lane & 4, y[1], y[0]));
    stx(rs, WS_P3 + ((((service ? 0 : 4) + (lane & 3)) * 16 + 2 * w + ((lane >> 3) & 1)) * 56 + f) * 128 + (lane & 4) * 16 + (lane >> 4) * 16, tag8(ys, t8));
    STAMP(14);
  }
  EXIT();
}

extern "C" void kernel_launch(void* const* d_in, const int* in_sizes, int n_in, void* d_out, int out_size, void* d_ws, size_t ws_size, hipStream_t stream) {
  (void)in_sizes; (void)n_in; (void)out_size; (void)ws_size;
  (void)hipFuncSetAttribute((const void*)fused_block, hipFuncAttributeMaxDynamicSharedMemorySize, LDS_ALLOC);
  fused_block<<<256, NTHREADS, LDS_ALLOC, stream>>>((const u16*)d_in[0], (const u16*)d_in[1], (const u16*)d_in[2], (const u16*)d_in[3], (const u16*)d_in[4],
                                                    (const u16*)d_in[6], (const u16*)d_in[7], (const u16*)d_in[8], (const u16*)d_in[9], (const u16*)d_in[10],
                                                    (u16*)d_out, (unsigned char*)d_ws);
}
```
